```python
import torch

def _softmax(x, axis=-1):
    return torch.softmax(x, dim=axis)
def _gelu(x):
    return torch.nn.functional.gelu(x, approximate="tanh")
def _silu(x):
    return torch.nn.functional.silu(x)
def _sigmoid(x):
    return torch.sigmoid(x)
def _layernorm(x, axis=-1, eps=1e-5):
    m = x.mean(dim=axis, keepdim=True)
    v = x.var(dim=axis, keepdim=True, unbiased=False)
    return (x - m) / torch.sqrt(v + eps)
def _scatter_add(tgt, idx, src, axis=0):
    return tgt.clone().index_add_(axis, idx, src)
def _segment_sum(src, seg, n):
    out = torch.zeros((n,) + src.shape[1:], dtype=src.dtype)
    return out.index_add_(0, seg, src)
def _pad(x, pw):
    flat = tuple(v for pair in reversed(pw) for v in pair)
    return torch.nn.functional.pad(x, flat)

kernel_name = 'standalone__gqa_block_cached__near_ridge'

def setup_inputs():

    _w = (lambda a: (lambda t: t.to(torch.bfloat16) if t.is_floating_point() else t)(torch.as_tensor(a)))
    B, Sq, D = 16, 32, 640
    Hq, Hkv, Sk, Dh = 32, 32, 32, 192
    assert Hq % Hkv == 0, (Hq, Hkv)
    return {
        "x": _w((lambda *s: torch.randn(*s, dtype=torch.bfloat16) * 0.8)(B, Sq, D)),
        "k_cache": _w((lambda *s: torch.randn(*s, dtype=torch.bfloat16) * 0.8)(B, Hkv, Sk, Dh)),
        "v_cache": _w((lambda *s: torch.randn(*s, dtype=torch.bfloat16) * 0.05)(B, Hkv, Sk, Dh)),
        "wq": _w((lambda *s: torch.randn(*s, dtype=torch.bfloat16) * 0.8)(D, Hq * Dh) * (D ** -0.5)),
        "wo": _w((lambda *s: torch.randn(*s, dtype=torch.bfloat16) * 0.05)(Hq * Dh, D) * ((Hq * Dh) ** -0.5)),
    }

def reference(x, k_cache, v_cache, wq, wo):
    B, Sq, D = x.shape
    Hq, Dh = 32, 192
    Hkv = k_cache.shape[1]
    rep = Hq // Hkv
    q = (lambda x, *d: x.permute(*d))((x @ wq).reshape(B, Sq, Hq, Dh), 0, 2, 1, 3)
    k = (lambda x, n, axis: torch.repeat_interleave(x, n, dim=axis))(k_cache, rep, axis=1)
    v = (lambda x, n, axis: torch.repeat_interleave(x, n, dim=axis))(v_cache, rep, axis=1)
    scores = torch.einsum("bhqd,bhkd->bhqk", q, k) * (Dh ** -0.5)
    p = _softmax(scores, axis=-1)
    o = torch.einsum("bhqk,bhkd->bhqd", p, v)
    return (lambda x, *d: x.permute(*d))(o, 0, 2, 1, 3).reshape(B, Sq, Hq * Dh) @ wo
```

```cpp
#include <hip/hip_runtime.h>
#include <hip/hip_bf16.h>
typedef unsigned short u16;
__device__ __forceinline__ float bf2f(u16 x){ return __uint_as_float(((unsigned)x)<<16); }
__device__ __forceinline__ u16 f2bf(float f){ unsigned u=__float_as_uint(f); u += 0x7fffu + ((u>>16)&1u); return (u16)(u>>16); }
constexpr int B=16,SQ=32,D=640,H=32,SK=32,DH=192;
__global__ void k1(const u16* x,const u16* kc,const u16* vc,const u16* wq,u16* o){
  __shared__ float q[SQ*DH];
  __shared__ float s[SQ*SK];
  int b=blockIdx.x/H,h=blockIdx.x%H;
  for(int i=threadIdx.x;i<SQ*DH;i+=blockDim.x){int r=i/DH,c=i%DH;float a=0;
    for(int k=0;k<D;k++)a+=bf2f(x[(b*SQ+r)*D+k])*bf2f(wq[(size_t)k*(H*DH)+h*DH+c]);
    q[i]=bf2f(f2bf(a));}
  __syncthreads();
  for(int i=threadIdx.x;i<SQ*SK;i+=blockDim.x){int r=i/SK,c=i%SK;float a=0;
    for(int k=0;k<DH;k++)a+=q[r*DH+k]*bf2f(kc[((size_t)(b*H+h)*SK+c)*DH+k]);
    a=bf2f(f2bf(a)); a=bf2f(f2bf(a*0.07216878364870322f));
    s[i]=a;}
  __syncthreads();
  if(threadIdx.x<SQ){int r=threadIdx.x;float m=-1e30f;for(int c=0;c<SK;c++)m=fmaxf(m,s[r*SK+c]);float l=0;
    for(int c=0;c<SK;c++){float e=__expf(s[r*SK+c]-m);s[r*SK+c]=e;l+=e;}
    for(int c=0;c<SK;c++)s[r*SK+c]=bf2f(f2bf(s[r*SK+c]/l));}
  __syncthreads();
  for(int i=threadIdx.x;i<SQ*DH;i+=blockDim.x){int r=i/DH,c=i%DH;float a=0;
    for(int k=0;k<SK;k++)a+=s[r*SK+k]*bf2f(vc[((size_t)(b*H+h)*SK+k)*DH+c]);
    o[(size_t)(b*SQ+r)*(H*DH)+h*DH+c]=f2bf(a);}
}
__global__ void k2(const u16* o,const u16* wo,u16* out){
  int i=blockIdx.x*blockDim.x+threadIdx.x; int r=i/D,c=i%D; float a=0;
  for(int k=0;k<H*DH;k++)a+=bf2f(o[(size_t)r*(H*DH)+k])*bf2f(wo[(size_t)k*D+c]);
  out[i]=f2bf(a);
}
extern "C" void kernel_launch(void* const* d_in, const int* in_sizes, int n_in,
                              void* d_out, int out_size, void* d_ws, size_t ws_size,
                              hipStream_t stream) {
  const u16* x=(const u16*)d_in[0];const u16* kc=(const u16*)d_in[1];const u16* vc=(const u16*)d_in[2];
  const u16* wq=(const u16*)d_in[3];const u16* wo=(const u16*)d_in[4];
  u16* o=(u16*)d_ws;
  k1<<<B*H,256,0,stream>>>(x,kc,vc,wq,o);
  k2<<<B*SQ*D/256,256,0,stream>>>(o,wo,(u16*)d_out);
}
```

```cpp
#include <hip/hip_runtime.h>
#include <hip/hip_bf16.h>
#include <stdint.h>

typedef unsigned u32;
typedef u32 v4u __attribute__((ext_vector_type(4)));
typedef u32 v2u __attribute__((ext_vector_type(2)));
typedef float f32x2 __attribute__((ext_vector_type(2)));
typedef float f32x4 __attribute__((ext_vector_type(4)));
typedef float f32x16 __attribute__((ext_vector_type(16)));
typedef short s16x4 __attribute__((ext_vector_type(4)));
typedef __bf16 bf16x2 __attribute__((ext_vector_type(2)));
typedef __bf16 bf16x8 __attribute__((ext_vector_type(8)));
#define LAS __attribute__((address_space(3)))
#define GAS __attribute__((address_space(1)))
#define DI __device__ __forceinline__
#define RLX_AGENT __ATOMIC_RELAXED, __HIP_MEMORY_SCOPE_AGENT

#define AUX_X 0
#define AUX_WQ 0
#define AUX_K 0
#define AUX_V 0
#define AUX_WO 0
#define ROT_G 0
#define ROT_H 0

namespace {
constexpr int SLOT = 32768, ACT = 24576, NSLOT = 5, LDS_BYTES = SLOT * NSLOT;
constexpr u32 WS_E = 0, WS_FLAGP = 4096, WS_FLAGO = 69632, WS_P = 262144, WS_SLAB = 2097152;
constexpr u32 WS_USED = WS_SLAB + 256 * 20480;
constexpr u32 SPIN_CAP = 1u << 18;

template <int AUX>
DI void dma(u32 voff, __amdgpu_buffer_rsrc_t rs, u32 soff, u32 ldsdst) {
  if constexpr (AUX == 0)
    asm volatile("s_mov_b32 m0, %2\n\ts_nop 0\n\tbuffer_load_dwordx4 %0, %1, %3 offen lds" ::"v"(voff), "s"(rs), "s"(ldsdst), "s"(soff) : "memory");
  else
    asm volatile("s_mov_b32 m0, %2\n\ts_nop 0\n\tbuffer_load_dwordx4 %0, %1, %3 offen nt lds" ::"v"(voff), "s"(rs), "s"(ldsdst), "s"(soff) : "memory");
}
template <int N> DI void wait_vm() { asm volatile("s_waitcnt vmcnt(%0)" ::"i"(N) : "memory"); }
DI void bar() {
  __builtin_amdgcn_sched_barrier(0);
  asm volatile("s_barrier" ::: "memory");
  __builtin_amdgcn_sched_barrier(0);
}
DI u32 rfl(u32 v) { return __builtin_amdgcn_readfirstlane(v); }
DI u32 pk2(float a, float b) {
  f32x2 v = {a, b};
  return __builtin_bit_cast(u32, __builtin_convertvector(v, bf16x2));
}
DI bf16x8 lds128(LAS char* p) { return *(LAS bf16x8*)p; }
DI bf16x8 ldstr(LAS char* p0, LAS char* p1) {
  s16x4 a = __builtin_amdgcn_ds_read_tr16_b64_v4i16((LAS s16x4*)p0);
  s16x4 b = __builtin_amdgcn_ds_read_tr16_b64_v4i16((LAS s16x4*)p1);
  return __builtin_bit_cast(bf16x8, __builtin_shufflevector(a, b, 0, 1, 2, 3, 4, 5, 6, 7));
}
DI f32x16 mfma32(bf16x8 a, bf16x8 b, f32x16 c) { return __builtin_amdgcn_mfma_f32_32x32x16_bf16(a, b, c, 0, 0, 0); }
DI f32x4 mfma16(bf16x8 a, bf16x8 b, f32x4 c) { return __builtin_amdgcn_mfma_f32_16x16x32_bf16(a, b, c, 0, 0, 0); }
#define STAMP(i)

DI void poll_eq(GAS u32* p, u32 want) {
  for (u32 spins = 0; spins < SPIN_CAP; ++spins) {
    u32 v = __hip_atomic_load(p, RLX_AGENT);
    if (__all(v == want)) break;
    __builtin_amdgcn_s_sleep(1);
  }
}
}

__global__ __launch_bounds__(512, 2) void gqa_block_kernel(const char* __restrict__ x, const char* __restrict__ kc, const char* __restrict__ vc,
                                                           const char* __restrict__ wq, const char* __restrict__ wo, char* __restrict__ out,
                                                           char* __restrict__ wsp) {
  extern __shared__ __attribute__((aligned(16))) char smem_[];
  LAS char* smem = (LAS char*)smem_;
  const int w = blockIdx.x;
  const int wave = rfl(threadIdx.x >> 6);
  const int lane = threadIdx.x & 63;
  const int j = w & 7, g = w >> 5, hh = (w >> 3) & 3;
  const int h = 4 * j + hh;
  GAS char* ws = (GAS char*)wsp;
  STAMP(0);

  if (wave >= 4) {
    const int lw = wave - 4;
    const __amdgpu_buffer_rsrc_t rX = __builtin_amdgcn_make_buffer_rsrc((void*)x, 0, 512 * 1280, 0x00020000);
    const __amdgpu_buffer_rsrc_t rWq = __builtin_amdgcn_make_buffer_rsrc((void*)wq, 0, 640 * 12288, 0x00020000);
    const __amdgpu_buffer_rsrc_t rK = __builtin_amdgcn_make_buffer_rsrc((void*)kc, 0, 512 * 12288, 0x00020000);
    const __amdgpu_buffer_rsrc_t rV = __builtin_amdgcn_make_buffer_rsrc((void*)vc, 0, 512 * 12288, 0x00020000);
    const __amdgpu_buffer_rsrc_t rWo = __builtin_amdgcn_make_buffer_rsrc((void*)wo, 0, 6144 * 1280, 0x00020000);
    u32 kk[6], cc[6];
#pragma unroll
    for (int i = 0; i < 6; ++i) {
      const u32 c = 64 * i + lane, k = c / 24, chp = c - 24 * k;
      kk[i] = k;
      cc[i] = 16 * (chp ^ (((k >> 1) & 1) << 2) ^ (((k >> 3) & 1) << 1));
    }
    u32 vW[6], vA[2];
#pragma unroll
    for (int i = 0; i < 6; ++i) vW[i] = kk[i] * 12288 + cc[i];
    u32 ra[2], ca[2];
#pragma unroll
    for (int i = 0; i < 2; ++i) {
      ra[i] = 8 * i + (lane >> 3);
      ca[i] = 16 * ((lane & 7) ^ ((ra[i] >> 1) & 7));
      vA[i] = ra[i] * 1280 + ca[i];
    }
    const u32 ldsW = lw * 6144, ldsA = ACT + lw * 2048;
    const int rot = (ROT_G * g + ROT_H * hh) % 10;
    u32 slot = 0;
    {
      const u32 soW0 = h * 384 + 16 * lw * 12288, soA0 = (64 * g + 16 * lw) * 1280;
      int kcur = rot;
#pragma unroll 1
      for (int t = 0; t < 10; ++t) {
        const u32 soW = soW0 + kcur * (64 * 12288), soA = soA0 + kcur * 128;
#pragma unroll
        for (int i = 0; i < 6; ++i) dma<AUX_WQ>(vW[i], rWq, soW, slot + ldsW + 1024 * i);
#pragma unroll
        for (int i = 0; i < 2; ++i) dma<AUX_X>(vA[i], rX, soA, slot + ldsA + 1024 * i);
        kcur = kcur == 9 ? 0 : kcur + 1;
        slot = slot == (NSLOT - 1) * SLOT ? 0 : slot + SLOT;
        if (t >= 3) {
          wait_vm<16>();
          bar();
        }
      }
    }
    STAMP(1);
    const int bl = 2 * g + (lw >> 1);
    const u32 soKV0 = (u32)(bl * 32 * 32 + 16 * (lw & 1)) * 384;
#pragma unroll
    for (int i = 0; i < 2; ++i) vA[i] = ra[i] * 384 + ca[i];
#pragma unroll
    for (int i = 0; i < 6; ++i) vW[i] = kk[i] * 1280 + cc[i];
    {
#pragma unroll
      for (int c = 0; c < 3; ++c)
#pragma unroll
        for (int i = 0; i < 2; ++i) dma<AUX_K>(vA[i], rK, soKV0 + h * 12288 + 128 * c, slot + 8192 * c + lw * 2048 + 1024 * i);
      slot += SLOT;
      wait_vm<14>();
      bar();
    }
    const u32 L0 = (5 * hh) >> 1;
    {
      int hi = 0, c = 0;
#pragma unroll 1
      for (int t = 11; t < 23; ++t) {
        const u32 hp = 4 * j + ((hh + hi) & 3);
        const u32 soW = (hp * 192 + 64 * c + 16 * lw) * 1280 + 128 * L0, soA = soKV0 + hp * 12288 + 128 * c;
#pragma unroll
        for (int i = 0; i < 6; ++i) dma<AUX_WO>(vW[i], rWo, soW, slot + ldsW + 1024 * i);
#pragma unroll
        for (int i = 0; i < 2; ++i) dma<AUX_V>(vA[i], rV, soA, slot + ldsA + 1024 * i);
        if (c == 2) { c = 0; ++hi; } else ++c;
        slot = slot == (NSLOT - 1) * SLOT ? 0 : slot + SLOT;
        if (t == 11) wait_vm<14>(); else wait_vm<16>();
        bar();
        if (t == 14) bar();
      }
    }
    wait_vm<8>();
    bar();
    wait_vm<0>();
    bar();
    STAMP(2);
    return;
  }

  const int cw = wave, b = cw >> 1, half = cw & 1;
  const u32 E = __hip_atomic_load((GAS u32*)(ws + WS_E + g * 128), RLX_AGENT);
  const __amdgpu_buffer_rsrc_t rWs = __builtin_amdgcn_make_buffer_rsrc((void*)wsp, 0, WS_USED, 0x00020000);

  f32x16 accQ[3];
#pragma unroll
  for (int i = 0; i < 3; ++i)
#pragma unroll
    for (int r = 0; r < 16; ++r) accQ[i][r] = 0.f;
  const int r32 = lane & 31, hl = lane >> 5;
  {
    const int blk = (lane >> 4) & 1, q4 = (lane & 15) >> 2, p = lane & 3;
    LAS char* aW[3];
#pragma unroll
    for (int tn = 0; tn < 3; ++tn)
      aW[tn] = smem + 384 * (8 * hl + q4) + 64 * ((3 * half + tn) ^ (q4 >> 1)) + 32 * (blk ^ hl) + 16 * (p & 1) + 8 * (p >> 1);
    LAS char* aX[4];
#pragma unroll
    for (int ks = 0; ks < 4; ++ks) aX[ks] = smem + ACT + 128 * (32 * b + r32) + 16 * ((2 * ks + hl) ^ ((r32 >> 1) & 7));

    bf16x8 wf[2][3], xf[2];
    auto rd1 = [&](int set, int slot, int ks) {
      xf[set] = lds128(aX[ks] + slot * SLOT);
#pragma unroll
      for (int tn = 0; tn < 3; ++tn) wf[set][tn] = ldstr(aW[tn] + slot * SLOT + 384 * 16 * ks, aW[tn] + slot * SLOT + 384 * (16 * ks + 4));
    };
    bar();
    STAMP(1);
    rd1(0, 0, 0);
#pragma unroll
    for (int s = 0; s < 10; ++s) {
#pragma unroll
      for (int ks = 0; ks < 4; ++ks) {
        if (ks < 3) rd1((ks + 1) & 1, s % NSLOT, ks + 1);
        else if (s < 9) rd1(0, (s + 1) % NSLOT, 0);
        __builtin_amdgcn_sched_barrier(0);
#pragma unroll
        for (int tn = 0; tn < 3; ++tn) {
          accQ[tn] = mfma32(wf[ks & 1][tn], xf[ks & 1], accQ[tn]);
        }
        __builtin_amdgcn_sched_barrier(0);
      }
      bar();
    }
  }
  STAMP(2);
  const u32 e = rfl(E) + 1;
  asm volatile("" ::"s"(e));
  {
    f32x16 accS;
#pragma unroll
    for (int r = 0; r < 16; ++r) accS[r] = 0.f;
    const int row = 32 * b + r32;
#pragma unroll
    for (int tn = 0; tn < 3; ++tn)
#pragma unroll
      for (int s = 0; s < 2; ++s) {
        const int ch = 12 * half + 4 * tn + 2 * s + hl;
        const bf16x8 kf = lds128(smem + 8192 * (ch >> 3) + 128 * row + 16 * ((ch & 7) ^ ((r32 >> 1) & 7)));
        v4u qp;
#pragma unroll
        for (int d = 0; d < 4; ++d) qp[d] = pk2(accQ[tn][8 * s + 2 * d], accQ[tn][8 * s + 2 * d + 1]);
        accS = mfma32(kf, __builtin_bit_cast(bf16x8, qp), accS);
      }
    LAS char* scr = smem + ACT + 4096 * b + 16 * lane;
    if (half == 1) {
#pragma unroll
      for (int i = 0; i < 4; ++i) *(LAS f32x4*)(scr + 1024 * i) = (f32x4){accS[4 * i], accS[4 * i + 1], accS[4 * i + 2], accS[4 * i + 3]};
      asm volatile("s_waitcnt lgkmcnt(0)" ::: "memory");
    }
    bar();
    if (half == 0) {
#pragma unroll
      for (int i = 0; i < 4; ++i) {
        const f32x4 o = *(LAS f32x4*)(scr + 1024 * i);
#pragma unroll
        for (int r = 0; r < 4; ++r) accS[4 * i + r] += o[r];
      }
      float m = accS[0];
#pragma unroll
      for (int r = 1; r < 16; ++r) m = fmaxf(m, accS[r]);
      m = fmaxf(m, __shfl_xor(m, 32));
      const float cs = 0.07216878364870322f * 1.4426950408889634f;
      float sum = 0.f;
#pragma unroll
      for (int r = 0; r < 16; ++r) {
        accS[r] = __builtin_amdgcn_exp2f((accS[r] - m) * cs);
        sum += accS[r];
      }
      sum += __shfl_xor(sum, 32);
      const float inv = 1.0f / sum;
      v4u f0, f1;
      f0[0] = pk2(accS[0] * inv, accS[1] * inv);   f0[1] = pk2(accS[2] * inv, accS[3] * inv);
      f0[2] = pk2(accS[8] * inv, accS[9] * inv);   f0[3] = pk2(accS[10] * inv, accS[11] * inv);
      f1[0] = pk2(accS[4] * inv, accS[5] * inv);   f1[1] = pk2(accS[6] * inv, accS[7] * inv);
      f1[2] = pk2(accS[12] * inv, accS[13] * inv); f1[3] = pk2(accS[14] * inv, accS[15] * inv);
      const u32 pb = WS_P + ((2 * g + b) * 32 + h) * 2048;
      const u32 vo = (r32 >> 4) * 1024 + ((r32 & 15) + 16 * hl) * 16;
      __builtin_amdgcn_raw_buffer_store_b128(f0, rWs, vo, pb, 16);
      __builtin_amdgcn_raw_buffer_store_b128(f1, rWs, vo + 512, pb, 16);
    }
    bar();
  }
  STAMP(3);
  {
    const int r16 = lane & 15, g4 = lane >> 4, q4 = (lane & 15) >> 2, p = lane & 3;
    const int c016 = (hh & 1) * 2;
    const int x2 = (g4 & 1) | ((q4 >> 1) << 1);
    LAS char* aW[5];
#pragma unroll
    for (int cb = 0; cb < 5; ++cb) aW[cb] = smem + 384 * (8 * g4 + q4) + 32 * ((c016 + 5 * half + cb) ^ x2) + 16 * (p >> 1) + 8 * (p & 1);
    LAS char* aV[2];
#pragma unroll
    for (int ks = 0; ks < 2; ++ks) aV[ks] = smem + ACT + 128 * (32 * b + r16) + 16 * ((4 * ks + g4) ^ ((r16 >> 1) & 7));
    bf16x8 wf[2][5], vf[2][2];
    auto rd2 = [&](int set, int slot, int ks) {
#pragma unroll
      for (int rb = 0; rb < 2; ++rb) vf[set][rb] = lds128(aV[ks] + slot * SLOT + 2048 * rb);
#pragma unroll
      for (int cb = 0; cb < 5; ++cb) wf[set][cb] = ldstr(aW[cb] + slot * SLOT + 384 * 32 * ks, aW[cb] + slot * SLOT + 384 * (32 * ks + 4));
    };
    f32x4 X[2][5];
    v4u Xp[4][5];
    v4u Pf[4][2];
    f32x4 O[5][2];
#pragma unroll
    for (int cb = 0; cb < 5; ++cb)
#pragma unroll
      for (int qb = 0; qb < 2; ++qb) O[cb][qb] = (f32x4){0.f, 0.f, 0.f, 0.f};
    auto pv = [&](int hi) {
#pragma unroll
      for (int cb = 0; cb < 5; ++cb)
#pragma unroll
        for (int qb = 0; qb < 2; ++qb)
          O[cb][qb] = mfma16(__builtin_bit_cast(bf16x8, Xp[hi][cb]), __builtin_bit_cast(bf16x8, Pf[hi][qb]), O[cb][qb]);
    };
    rd2(0, 11 % NSLOT, 0);
#pragma unroll
    for (int hi = 0; hi < 4; ++hi) {
#pragma unroll
      for (int rb = 0; rb < 2; ++rb)
#pragma unroll
        for (int cb = 0; cb < 5; ++cb) X[rb][cb] = (f32x4){0.f, 0.f, 0.f, 0.f};
#pragma unroll
      for (int c = 0; c < 3; ++c) {
        const int s = 11 + 3 * hi + c;
#pragma unroll
        for (int ks = 0; ks < 2; ++ks) {
          if (ks == 0) rd2(1, s % NSLOT, 1);
          else if (s < 22) rd2(0, (s + 1) % NSLOT, 0);
          __builtin_amdgcn_sched_barrier(0);
#pragma unroll
          for (int rb = 0; rb < 2; ++rb)
#pragma unroll
            for (int cb = 0; cb < 5; ++cb) {
              X[rb][cb] = mfma16(vf[ks][rb], wf[ks][cb], X[rb][cb]);
            }
          __builtin_amdgcn_sched_barrier(0);
        }
        if (s == 12 && half == 0) {
          wait_vm<0>();
          if (lane == 0) __hip_atomic_store((GAS u32*)(ws + WS_FLAGP + ((2 * g + b) * 32 + h) * 128), e, RLX_AGENT);
        }
        if (s == 16) {
          const u32 hp = 4 * j + ((hh + (lane & 3)) & 3);
          poll_eq((GAS u32*)(ws + WS_FLAGP + ((2 * g + b) * 32 + hp) * 128), e);
          asm volatile("" ::: "memory");
#pragma unroll
          for (int i = 0; i < 4; ++i) {
            const u32 pb = WS_P + ((2 * g + b) * 32 + 4 * j + ((hh + i) & 3)) * 2048;
#pragma unroll
            for (int qb = 0; qb < 2; ++qb) Pf[i][qb] = __builtin_amdgcn_raw_buffer_load_b128(rWs, lane * 16 + 1024 * qb, pb, 16);
          }
        }
        if (c == 2) {
#pragma unroll
          for (int cb = 0; cb < 5; ++cb) {
            Xp[hi][cb][0] = pk2(X[0][cb][0], X[0][cb][1]);
            Xp[hi][cb][1] = pk2(X[0][cb][2], X[0][cb][3]);
            Xp[hi][cb][2] = pk2(X[1][cb][0], X[1][cb][1]);
            Xp[hi][cb][3] = pk2(X[1][cb][2], X[1][cb][3]);
          }
          if (hi == 2) { pv(0); pv(1); pv(2); }
          if (hi == 3) pv(3);
        }
        if (s <= 20) bar();
      }
    }
    STAMP(4);
    const u32 pos = (r16 >> 1) * 8 + (r16 & 1) * 4 + g4;
    {
      const u32 sb = WS_SLAB + (w * 4 + cw) * 5120;
#pragma unroll
      for (int cb = 0; cb < 5; ++cb) {
        v4u v;
        v[0] = pk2(O[cb][0][0], O[cb][0][1]); v[1] = pk2(O[cb][0][2], O[cb][0][3]);
        v[2] = pk2(O[cb][1][0], O[cb][1][1]); v[3] = pk2(O[cb][1][2], O[cb][1][3]);
        __builtin_amdgcn_raw_buffer_store_b128(v, rWs, pos * 16 + 1024 * cb, sb, 16);
      }
      wait_vm<0>();
      if (lane == 0) __hip_atomic_store((GAS u32*)(ws + WS_FLAGO + (w * 4 + cw) * 128), e, RLX_AGENT);
    }
    STAMP(5);
    {
      const int w0 = w & ~7;
      poll_eq((GAS u32*)(ws + WS_FLAGO + ((w0 + (lane & 7)) * 4 + cw) * 128), e);
      asm volatile("" ::: "memory");
      STAMP(6);
      if (lane < 40) {
        const int cb = lane >> 3, rr = lane & 7;
        const u32 vo = (8 * j + rr) * 16 + 1024 * cb;
        v4u v[8];
#pragma unroll
        for (int i = 0; i < 8; ++i) v[i] = __builtin_amdgcn_raw_buffer_load_b128(rWs, vo, WS_SLAB + ((w0 + i) * 4 + cw) * 5120, 16);
        float a[8];
#pragma unroll
        for (int d = 0; d < 4; ++d) {
          float lo = 0.f, hi = 0.f;
#pragma unroll
          for (int i = 0; i < 8; ++i) {
            lo += __uint_as_float(v[i][d] << 16);
            hi += __uint_as_float(v[i][d] & 0xffff0000u);
          }
          a[2 * d] = lo; a[2 * d + 1] = hi;
        }
        const int qp = 2 * j + (rr >> 2), gg = rr & 3;
        const u32 col = 160 * hh + 80 * half + 16 * cb + 4 * gg;
        GAS char* o0 = (GAS char*)out + (size_t)((2 * g + b) * 32 + qp) * 1280 + col * 2;
        *(GAS v2u*)o0 = (v2u){pk2(a[0], a[1]), pk2(a[2], a[3])};
        *(GAS v2u*)(o0 + 16 * 1280) = (v2u){pk2(a[4], a[5]), pk2(a[6], a[7])};
      }
      if (j == 0 && hh == 0 && cw == 0 && lane == 0) __hip_atomic_store((GAS u32*)(ws + WS_E + g * 128), e, RLX_AGENT);
    }
    STAMP(7);
  }
}

extern "C" void kernel_launch(void* const* d_in, const int* in_sizes, int n_in, void* d_out, int out_size, void* d_ws, size_t ws_size,
                              hipStream_t stream) {
  hipFuncSetAttribute((const void*)gqa_block_kernel, hipFuncAttributeMaxDynamicSharedMemorySize, LDS_BYTES);
  gqa_block_kernel<<<256, 512, LDS_BYTES, stream>>>((const char*)d_in[0], (const char*)d_in[1], (const char*)d_in[2], (const char*)d_in[3],
                                                     (const char*)d_in[4], (char*)d_out, (char*)d_ws);
}
```

```cpp
#include <hip/hip_runtime.h>
#include <hip/hip_bf16.h>
#include <stdint.h>

typedef unsigned u32;
typedef u32 v4u __attribute__((ext_vector_type(4)));
typedef u32 v2u __attribute__((ext_vector_type(2)));
typedef float f32x2 __attribute__((ext_vector_type(2)));
typedef float f32x4 __attribute__((ext_vector_type(4)));
typedef short s16x4 __attribute__((ext_vector_type(4)));
typedef __bf16 bf16x2 __attribute__((ext_vector_type(2)));
typedef __bf16 bf16x8 __attribute__((ext_vector_type(8)));
#define LAS __attribute__((address_space(3)))
#define GAS __attribute__((address_space(1)))
#define DI __device__ __forceinline__
#define RLX_AGENT __ATOMIC_RELAXED, __HIP_MEMORY_SCOPE_AGENT
#define SB() __builtin_amdgcn_sched_barrier(0)

namespace {
constexpr int SLOT = 32768, ACT = 24576, NSLOT = 4, KREG = 0, SCR = 24576, RING = 32768, LDS_BYTES = RING + SLOT * NSLOT;
constexpr int NST = 22;
constexpr u32 WS_E = 0, WS_FLAGP = 4096, WS_FLAGO = 135168, WS_P = 266240, WS_SLAB = 1314816;
constexpr u32 WS_USED = WS_SLAB + 256 * 20480;
constexpr u32 SPIN_CAP = 1u << 18;
constexpr int LA = 8;
constexpr int EPO_N = 4;
constexpr int FDMA_S = 10;
constexpr int PDMA_S = 15;
constexpr int PCHK_N = 14;

template <bool SC1>
DI void dma(u32 voff, __amdgpu_buffer_rsrc_t rs, u32 soff, u32 ldsdst) {
  if constexpr (SC1)
    asm volatile("s_mov_b32 m0, %2\n\ts_nop 0\n\tbuffer_load_dwordx4 %0, %1, %3 offen sc1 lds" ::"v"(voff), "s"(rs), "s"(ldsdst), "s"(soff) : "memory");
  else
    asm volatile("s_mov_b32 m0, %2\n\ts_nop 0\n\tbuffer_load_dwordx4 %0, %1, %3 offen lds" ::"v"(voff), "s"(rs), "s"(ldsdst), "s"(soff) : "memory");
}
DI void dma_word(u32 voff, __amdgpu_buffer_rsrc_t rs, u32 soff, u32 ldsdst) {
  asm volatile("s_mov_b32 m0, %2\n\ts_nop 0\n\tbuffer_load_dword %0, %1, %3 offen sc1 lds" ::"v"(voff), "s"(rs), "s"(ldsdst), "s"(soff) : "memory");
}
template <int N> DI void wait_vm() { asm volatile("s_waitcnt vmcnt(%0)" ::"i"(N) : "memory"); }
DI void bar() {
  SB();
  asm volatile("s_barrier" ::: "memory");
  SB();
}
DI void wait_lgkm0() {
  asm volatile("" ::: "memory");
  __builtin_amdgcn_s_waitcnt(0xC07F);
  asm volatile("" ::: "memory");
}
DI u32 rfl(u32 v) { return __builtin_amdgcn_readfirstlane(v); }
DI u32 pk2(float a, float b) {
  f32x2 v = {a, b};
  return __builtin_bit_cast(u32, __builtin_convertvector(v, bf16x2));
}
DI LAS char* lp(u32 a) { return (LAS char*)a; }
DI bf16x8 lds128(u32 a, int off) { return *(LAS bf16x8*)(lp(a) + off); }
DI s16x4 ldstr4(u32 a, int off) { return __builtin_amdgcn_ds_read_tr16_b64_v4i16((LAS s16x4*)(lp(a) + off)); }
DI bf16x8 cat8(s16x4 a, s16x4 b) { return __builtin_bit_cast(bf16x8, __builtin_shufflevector(a, b, 0, 1, 2, 3, 4, 5, 6, 7)); }
DI f32x4 mfma16(bf16x8 a, bf16x8 b, f32x4 c) { return __builtin_amdgcn_mfma_f32_16x16x32_bf16(a, b, c, 0, 0, 0); }

template <int V> struct IC { static constexpr int value = V; };
template <int I, int N, class F> DI void static_for(F&& f) {
  if constexpr (I < N) {
    f(IC<I>{});
    static_for<I + 1, N>(f);
  }
}
constexpr int rsched1(int ks, int m) { return ks == 0 ? (m <= 7 ? 2 * m : 14) : (m == 0 ? 0 : (m <= 4 ? m + 1 : (m <= 12 ? m + 2 : 14))); }
constexpr int rsched2(int ks, int m) { return ks == 0 ? (m <= 4 ? 2 * m : (m <= 8 ? m + 4 : 12)) : (m == 0 ? 0 : (m <= 4 ? m + 1 : m + 2)); }
constexpr int pvsched(int n) {
  if (n <= PCHK_N) return 0;
  if (n <= 18) { const int a = (20 * (n - PCHK_N - 1) + (17 - PCHK_N)) / (17 - PCHK_N); return a > 20 ? 20 : a; }
  if (n <= 23) return 20 + 2 * (n - 18);
  return 40;
}
static_assert(pvsched(18) == 20 && pvsched(23) == 30 && pvsched(24) == 40 && PCHK_N >= 11 && PCHK_N <= 16, "");
static_assert(2 * (PDMA_S + 1 - 10) - 1 <= PCHK_N - 1 && FDMA_S < PDMA_S && FDMA_S >= 10, "flags before fragments, both landed before they are read");

DI void load8_sc1(v4u (&v)[8], u32 vo, __amdgpu_buffer_rsrc_t rs, u32 s0, u32 step) {
  u32 s1 = s0 + step, s2 = s1 + step, s3 = s2 + step, s4 = s3 + step, s5 = s4 + step, s6 = s5 + step, s7 = s6 + step;
  asm volatile(
      "s_nop 4\n\t"
      "buffer_load_dwordx4 %0, %8, %9, %10 offen sc1\n\t"
      "buffer_load_dwordx4 %1, %8, %9, %11 offen sc1\n\t"
      "buffer_load_dwordx4 %2, %8, %9, %12 offen sc1\n\t"
      "buffer_load_dwordx4 %3, %8, %9, %13 offen sc1\n\t"
      "buffer_load_dwordx4 %4, %8, %9, %14 offen sc1\n\t"
      "buffer_load_dwordx4 %5, %8, %9, %15 offen sc1\n\t"
      "buffer_load_dwordx4 %6, %8, %9, %16 offen sc1\n\t"
      "buffer_load_dwordx4 %7, %8, %9, %17 offen sc1\n\t"
      "s_waitcnt vmcnt(0)"
      : "=&v"(v[0]), "=&v"(v[1]), "=&v"(v[2]), "=&v"(v[3]), "=&v"(v[4]), "=&v"(v[5]), "=&v"(v[6]), "=&v"(v[7])
      : "v"(vo), "s"(rs), "s"(s0), "s"(s1), "s"(s2), "s"(s3), "s"(s4), "s"(s5), "s"(s6), "s"(s7)
      : "memory");
}
DI void load4x2_sc1(v4u (&v)[8], u32 vo, __amdgpu_buffer_rsrc_t rs, u32 s0, u32 s1, u32 s2, u32 s3) {
  asm volatile(
      "s_nop 4\n\t"
      "buffer_load_dwordx4 %0, %8, %9, %10 offen sc1\n\t"
      "buffer_load_dwordx4 %1, %8, %9, %10 offen offset:1024 sc1\n\t"
      "buffer_load_dwordx4 %2, %8, %9, %11 offen sc1\n\t"
      "buffer_load_dwordx4 %3, %8, %9, %11 offen offset:1024 sc1\n\t"
      "buffer_load_dwordx4 %4, %8, %9, %12 offen sc1\n\t"
      "buffer_load_dwordx4 %5, %8, %9, %12 offen offset:1024 sc1\n\t"
      "buffer_load_dwordx4 %6, %8, %9, %13 offen sc1\n\t"
      "buffer_load_dwordx4 %7, %8, %9, %13 offen offset:1024 sc1\n\t"
      "s_waitcnt vmcnt(0)"
      : "=&v"(v[0]), "=&v"(v[1]), "=&v"(v[2]), "=&v"(v[3]), "=&v"(v[4]), "=&v"(v[5]), "=&v"(v[6]), "=&v"(v[7])
      : "v"(vo), "s"(rs), "s"(s0), "s"(s1), "s"(s2), "s"(s3)
      : "memory");
}
DI void poll_eq(GAS u32* p, u32 want) {
  for (u32 spins = 0; spins < SPIN_CAP; ++spins) {
    const u32 v = __hip_atomic_load(p, RLX_AGENT);
    if (__all(v == want)) break;
    __builtin_amdgcn_s_sleep(1);
  }
}
DI bool all_written(const v4u (&v)[8]) {
  u32 mx = 0;
#pragma unroll
  for (int i = 0; i < 8; ++i) {
    const u32 a = v[i][0] > v[i][1] ? v[i][0] : v[i][1], c = v[i][2] > v[i][3] ? v[i][2] : v[i][3];
    const u32 d = a > c ? a : c;
    mx = mx > d ? mx : d;
  }
  return __all(mx != 0xffffffffu);
}
}

__global__ __launch_bounds__(512, 2) void gqa_block_kernel(const char* __restrict__ x, const char* __restrict__ kc, const char* __restrict__ vc,
                                                           const char* __restrict__ wq, const char* __restrict__ wo, char* __restrict__ out,
                                                           char* wsp) {
  extern __shared__ __attribute__((aligned(16))) char smem_[];
  const int w = blockIdx.x;
  const int wave = rfl(threadIdx.x >> 6);
  const int lane = threadIdx.x & 63;
  const int j = w & 7, g = w >> 5, hh = (w >> 3) & 3;
  const int h = 4 * j + hh;
  const int hrot = hh + g;
  GAS char* ws = (GAS char*)wsp;
  const u32 lbase = (u32)(size_t)(LAS char*)smem_;

  auto loader = [&]() __attribute__((always_inline)) {
    const int lw = wave - 4;
    __builtin_amdgcn_s_setprio(1);
    const __amdgpu_buffer_rsrc_t rX = __builtin_amdgcn_make_buffer_rsrc((void*)x, 0, 512 * 1280, 0x00020000);
    const __amdgpu_buffer_rsrc_t rWq = __builtin_amdgcn_make_buffer_rsrc((void*)wq, 0, 640 * 12288, 0x00020000);
    u32 ra[2], ca[2], vA[2], vKV[2];
#pragma unroll
    for (int i = 0; i < 2; ++i) {
      ra[i] = 8 * i + (lane >> 3);
      ca[i] = 16 * ((lane & 7) ^ ((ra[i] >> 1) & 7));
      vA[i] = ra[i] * 1280 + ca[i];
    }
    u32 kk[6], cc[6], vW[6];
#pragma unroll
    for (int i = 0; i < 6; ++i) {
      const u32 L = 8 * i + (lane >> 3), k = (L * 43) >> 7;
      kk[i] = k;
      cc[i] = 128 * (L - 3 * k) + 16 * ((lane & 7) ^ ((k & 2) << 1) ^ ((k & 8) >> 2));
      vW[i] = k * 12288 + cc[i];
    }
#pragma unroll
    for (int i = 0; i < 2; ++i) vKV[i] = ra[i] * 384 + ca[i];
    const u32 ldsW = lbase + RING + lw * 6144, ldsA = lbase + RING + ACT + lw * 2048;
    const u32 soW0 = h * 384 + 16 * lw * 12288, soA0 = (64 * g + 16 * lw) * 1280;
    const int bl = 2 * g + (lw >> 1);
    const u32 soKV0 = (u32)(bl * 32 * 32 + 16 * (lw & 1)) * 384;
    const u32 L0 = (5 * hh) >> 1;
    int kcur = (g + 3 * hh) % 10;
    const int crot = g >> 2;
    const int kpos = ((w >> 3) + j) & 7;
    const __amdgpu_buffer_rsrc_t rWs = __builtin_amdgcn_make_buffer_rsrc((void*)wsp, 0, WS_USED, 0x00020000);
    u32 Ev = 0;
    auto after_barrier = [&](int s) {
      if (s == 0) {
        if (lw == 0) dma_word(0, rWs, WS_E + g * 128, lbase + SCR);
        asm volatile("buffer_load_dword %0, %1, %2, %3 offen sc1" : "+v"(Ev) : "v"(0u), "s"(rWs), "s"(WS_E + g * 128) : "memory");
        const v4u ones = {~0u, ~0u, ~0u, ~0u};
        asm volatile("buffer_store_dwordx4 %0, %1, %2, %3 offen sc1\n\ts_nop 1" ::"v"(ones), "v"(lane * 16 + 1024 * (lw & 1)), "s"(rWs),
                     "s"(WS_P + (bl * 32 + h) * 2048)
                     : "memory");
      }
      if (s == 1) {
        asm volatile("" : "+v"(Ev));
        const u32 ev = Ev + 1;
        if (lane == 0)
          asm volatile("buffer_store_dword %0, %1, %2, %3 offen sc1\n\ts_nop 1" ::"v"(ev), "v"(0u), "s"(rWs),
                       "s"(WS_FLAGP + ((bl * 32 + h) * 2 + (lw & 1)) * 128)
                       : "memory");
      }
      if (s == FDMA_S && lw == 0)
        dma_word((((lane >> 3) & 1) * 64 + (lane & 7)) * 128, rWs, WS_FLAGP + ((2 * g * 32 + 4 * j) * 2) * 128, lbase + KREG + 16384);
      if (s == PDMA_S) {
#pragma unroll
        for (int i = 0; i < 2; ++i)
#pragma unroll
          for (int qb = 0; qb < 2; ++qb) {
            const int hx = 2 * (lw & 1) + i;
            dma<true>(lane * 16, rWs, WS_P + (bl * 32 + 4 * j + ((hrot + hx) & 3)) * 2048 + 1024 * qb,
                      lbase + KREG + (((lw >> 1) * 4 + hx) * 2 + qb) * 1024);
          }
      }
      if (s < 8 && s == kpos) {
        const __amdgpu_buffer_rsrc_t rK = __builtin_amdgcn_make_buffer_rsrc((void*)kc, 0, 512 * 12288, 0x00020000);
#pragma unroll
        for (int c = 0; c < 3; ++c)
#pragma unroll
          for (int i = 0; i < 2; ++i) dma<false>(vKV[i], rK, soKV0 + h * 12288 + 128 * c, lbase + KREG + 8192 * c + lw * 2048 + 1024 * i);
      }
    };
    static_for<0, NST>([&](auto tc_) {
      constexpr int t = decltype(tc_)::value;
      constexpr u32 slot = (t % NSLOT) * SLOT;
      u32 soW, soA;
      if constexpr (t < 10) {
        soW = soW0 + kcur * (64 * 12288);
        soA = soA0 + kcur * 128;
        kcur = kcur == 9 ? 0 : kcur + 1;
      } else {
        if constexpr (t == 10) {
#pragma unroll
          for (int i = 0; i < 2; ++i) vA[i] = vKV[i];
#pragma unroll
          for (int i = 0; i < 6; ++i) vW[i] = kk[i] * 1280 + cc[i];
        }
        constexpr int hi = (t - 10) / 3, c0 = (t - 10) % 3;
        const int c = c0 + crot >= 3 ? c0 + crot - 3 : c0 + crot;
        const u32 hp = 4 * j + ((hrot + hi) & 3);
        soW = (hp * 192 + 64 * c + 16 * lw) * 1280 + 128 * L0;
        soA = soKV0 + hp * 12288 + 128 * c;
      }
      static_for<0, 8>([&](auto ic_) {
        constexpr int i = decltype(ic_)::value, p = 8 * t + i;
        if constexpr (t < 10) {
          if constexpr (i < 2) dma<false>(vA[i], rX, soA, slot + ldsA + 1024 * i);
          else dma<false>(vW[i - 2], rWq, soW, slot + ldsW + 1024 * (i - 2));
        } else {
          const __amdgpu_buffer_rsrc_t rV = __builtin_amdgcn_make_buffer_rsrc((void*)vc, 0, 512 * 12288, 0x00020000);
          const __amdgpu_buffer_rsrc_t rWo = __builtin_amdgcn_make_buffer_rsrc((void*)wo, 0, 6144 * 1280, 0x00020000);
          if constexpr (i < 2) dma<false>(vA[i], rV, soA, slot + ldsA + 1024 * i);
          else dma<false>(vW[i - 2], rWo, soW, slot + ldsW + 1024 * (i - 2));
        }
        if constexpr (p + 1 - LA >= 8 && (p + 1 - LA) % 8 == 0) {
          constexpr int s = (p + 1 - LA) / 8 - 1;
          wait_vm<LA>();
          bar();
          if constexpr (s < 8 || s == FDMA_S || s == PDMA_S) after_barrier(s);
        }
      });
    });
    static_for<(8 * NST - LA) / 8, NST>([&](auto sc_) {
      constexpr int s = decltype(sc_)::value;
      wait_vm<8 * (NST - 1 - s)>();
      bar();
    });
  };

  auto compute = [&]() __attribute__((always_inline)) {
    const int cw = wave, b = cw >> 1, half = cw & 1;
    const __amdgpu_buffer_rsrc_t rWs = __builtin_amdgcn_make_buffer_rsrc((void*)wsp, 0, WS_USED, 0x00020000);
    const int r16 = lane & 15, g4 = lane >> 4, q4 = (lane & 15) >> 2, p4 = lane & 3;
    const int x2 = (g4 & 1) | ((q4 >> 1) << 1);
    const u32 fsw = (r16 >> 1) & 7;
    u32 e = 0, Ev = 0;
    f32x4 accS[2][2];
    {
      f32x4 accQ[6][2];
      s16x4 kl[3][2], kh[3][2];
      u32 aW[6], aX[2], aY[2], aK[3][2];
#pragma unroll
      for (int tb = 0; tb < 6; ++tb) aW[tb] = lbase + RING + 384 * (8 * g4 + q4) + 32 * ((6 * half + tb) ^ x2) + 16 * (p4 >> 1) + 8 * (p4 & 1);
#pragma unroll
      for (int ks = 0; ks < 2; ++ks) {
        aX[ks] = lbase + RING + ACT + 128 * (32 * b + 16 * half + r16) + 16 * ((4 * ks + g4) ^ fsw);
        aY[ks] = lbase + RING + ACT + 128 * (32 * b + 16 * (half ^ 1) + r16) + 16 * ((4 * ks + g4) ^ fsw);
      }
#pragma unroll
      for (int u = 0; u < 3; ++u)
#pragma unroll
        for (int a = 0; a < 2; ++a) {
          const int ch = 12 * half + 4 * u + 2 * a + (g4 >> 1);
          aK[u][a] = lbase + KREG + 8192 * (ch >> 3) + 128 * (32 * b + r16) + 16 * ((ch & 7) ^ fsw) + 8 * (g4 & 1);
        }
#pragma unroll
      for (int i = 0; i < 6; ++i) asm volatile("" : "+v"(aW[i]));
#pragma unroll
      for (int i = 0; i < 2; ++i) asm volatile("" : "+v"(aX[i]), "+v"(aY[i]));
#pragma unroll
      for (int i = 0; i < 6; ++i) asm volatile("" : "+v"(aK[i >> 1][i & 1]));

      s16x4 wl[2][6], wh[2][6];
      bf16x8 xf[2][2];
      auto rd1 = [&](int n, int part) {
        const int set = n & 1, ks = n & 1;
        if (part == 0) xf[set][0] = lds128(aX[ks], 0);
        else if (part == 3) xf[set][1] = lds128(aY[ks], 0);
        else {
          const int q = part < 3 ? part - 1 : part - 2, tb = q >> 1;
          if (q & 1) wh[set][tb] = ldstr4(aW[tb], 384 * (32 * ks + 4));
          else wl[set][tb] = ldstr4(aW[tb], 384 * 32 * ks);
        }
      };
      auto rdk = [&](int m) {
        const int u = m >> 2, kb = (m >> 1) & 1, a = m & 1;
        const s16x4 v = *(LAS s16x4*)(lp(aK[u][a]) + 2048 * kb);
        if (a) kh[u][kb] = v; else kl[u][kb] = v;
      };
      auto adv1 = [&](int s) {
        const u32 d = (s % NSLOT == 0) ? (u32)(-(NSLOT - 1) * SLOT) : (u32)SLOT;
#pragma unroll
        for (int i = 0; i < 6; ++i) { aW[i] += d; asm volatile("" : "+v"(aW[i])); }
#pragma unroll
        for (int i = 0; i < 2; ++i) { aX[i] += d; aY[i] += d; asm volatile("" : "+v"(aX[i]), "+v"(aY[i])); }
      };
      auto mm1 = [&](int n, int tb, int qb) {
        const int set = n & 1;
        const f32x4 c0 = (n == 0) ? (f32x4){0.f, 0.f, 0.f, 0.f} : accQ[tb][qb];
        accQ[tb][qb] = mfma16(cat8(wl[set][tb], wh[set][tb]), xf[set][qb], c0);
      };
      bar();
#pragma unroll
      for (int q = 0; q < 14; ++q) rd1(0, q);
      SB();
#pragma unroll
      for (int n = 0; n < 19; ++n) {
        const int s = n >> 1, ks = n & 1;
        if (ks == 1) {
          wait_lgkm0();
          bar();
        }
#pragma unroll
        for (int m = 0; m < 12; ++m) {
          mm1(n, m >> 1, m & 1);
          SB();
#pragma unroll
          for (int q = 0; q < 14; ++q)
            if (q >= rsched1(ks, m) && q < rsched1(ks, m + 1)) rd1(n + 1, q);
          if (ks == 0 && s < 9 && m == 9) adv1(s + 1);
          SB();
        }
        if (n == EPO_N - 2) Ev = *(LAS u32*)(lp(lbase + SCR));
        if (n == EPO_N) {
          e = rfl(Ev) + 1;
          asm volatile("" : "+s"(e));
        }
        SB();
      }
      v4u qp[2];
      auto qpack = [&](int u, int qb) {
        qp[qb][0] = pk2(accQ[2 * u][qb][0], accQ[2 * u][qb][1]);
        qp[qb][1] = pk2(accQ[2 * u][qb][2], accQ[2 * u][qb][3]);
        qp[qb][2] = pk2(accQ[2 * u + 1][qb][0], accQ[2 * u + 1][qb][1]);
        qp[qb][3] = pk2(accQ[2 * u + 1][qb][2], accQ[2 * u + 1][qb][3]);
      };
      auto smm = [&](int u) {
#pragma unroll
        for (int qb = 1; qb >= 0; --qb)
#pragma unroll
          for (int kb = 0; kb < 2; ++kb) {
            const f32x4 c0 = u == 0 ? (f32x4){0.f, 0.f, 0.f, 0.f} : accS[kb][qb];
            accS[kb][qb] = mfma16(cat8(kl[u][kb], kh[u][kb]), __builtin_bit_cast(bf16x8, qp[qb]), c0);
          }
      };
#pragma unroll
      for (int m = 0; m < 12; ++m) {
        mm1(19, m >> 1, m & 1);
        SB();
        if (m < 6) { rdk(2 * m); rdk(2 * m + 1); }
        if (m == 5) qpack(0, 0);
        if (m == 6) qpack(0, 1);
        if (m == 7) smm(0);
        if (m == 9) qpack(1, 0);
        if (m == 10) qpack(1, 1);
        if (m == 11) smm(1);
        SB();
      }
      qpack(2, 0);
      qpack(2, 1);
      smm(2);
      const u32 scw = lbase + SCR + 4096 * b + 2048 * half + 16 * lane;
#pragma unroll
      for (int kb = 0; kb < 2; ++kb) *(LAS f32x4*)(lp(scw) + 1024 * kb) = accS[kb][1];
      wait_lgkm0();
    }
    const int c016 = (hh & 1) * 2;
    u32 aW[5], aV[2];
#pragma unroll
    for (int cb = 0; cb < 5; ++cb)
      aW[cb] = lbase + RING + SLOT * (10 % NSLOT) + 384 * (8 * g4 + q4) + 32 * ((c016 + 5 * half + cb) ^ x2) + 16 * (p4 >> 1) + 8 * (p4 & 1);
#pragma unroll
    for (int ks = 0; ks < 2; ++ks) aV[ks] = lbase + RING + SLOT * (10 % NSLOT) + ACT + 128 * (32 * b + r16) + 16 * ((4 * ks + g4) ^ fsw);
#pragma unroll
    for (int i = 0; i < 5; ++i) asm volatile("" : "+v"(aW[i]));
#pragma unroll
    for (int i = 0; i < 2; ++i) asm volatile("" : "+v"(aV[i]));
    bar();
    s16x4 wl[2][5], wh[2][5];
    bf16x8 vf[2][2];
    auto rd2 = [&](int n, int part) {
      const int set = n & 1, ks = n & 1;
      if (part == 0) vf[set][0] = lds128(aV[ks], 0);
      else if (part == 3) vf[set][1] = lds128(aV[ks], 2048);
      else {
        const int q = part < 3 ? part - 1 : part - 2, cb = q >> 1;
        if (q & 1) wh[set][cb] = ldstr4(aW[cb], 384 * (32 * ks + 4));
        else wl[set][cb] = ldstr4(aW[cb], 384 * 32 * ks);
      }
    };
    auto adv2 = [&](int s) {
      const u32 d = (s % NSLOT == 0) ? (u32)(-(NSLOT - 1) * SLOT) : (u32)SLOT;
#pragma unroll
      for (int i = 0; i < 5; ++i) { aW[i] += d; asm volatile("" : "+v"(aW[i])); }
#pragma unroll
      for (int i = 0; i < 2; ++i) { aV[i] += d; asm volatile("" : "+v"(aV[i])); }
    };
    f32x4 X[2][5];
    v4u Xp[4][5];
    v4u Pf[8];
    f32x4 O[5][2];
#pragma unroll
    for (int cb = 0; cb < 5; ++cb)
#pragma unroll
      for (int qb = 0; qb < 2; ++qb) O[cb][qb] = (f32x4){0.f, 0.f, 0.f, 0.f};
    auto mm2 = [&](int n, int cb, int rb) {
      const int set = n & 1;
      const f32x4 c0 = (n % 6 == 0) ? (f32x4){0.f, 0.f, 0.f, 0.f} : X[rb][cb];
      X[rb][cb] = mfma16(vf[set][rb], cat8(wl[set][cb], wh[set][cb]), c0);
    };
    auto pack = [&](int hi, int cb) {
      Xp[hi][cb][0] = pk2(X[0][cb][0], X[0][cb][1]);
      Xp[hi][cb][1] = pk2(X[0][cb][2], X[0][cb][3]);
      Xp[hi][cb][2] = pk2(X[1][cb][0], X[1][cb][1]);
      Xp[hi][cb][3] = pk2(X[1][cb][2], X[1][cb][3]);
      asm volatile("" : "+v"(Xp[hi][cb]));
    };
    auto pvi = [&](int k) {
      const int hi = k / 10, cb = (k % 10) >> 1, qb = k & 1;
      O[cb][qb] = mfma16(__builtin_bit_cast(bf16x8, Xp[hi][cb]), __builtin_bit_cast(bf16x8, Pf[2 * hi + qb]), O[cb][qb]);
    };
    float sv[8], mc = 0.f, sum = 0.f, t0 = 0.f, t1 = 0.f;
    v4u pfo;
    constexpr float cs = 0.07216878364870322f * 1.4426950408889634f;
    {
      const u32 scr = lbase + SCR + 4096 * b + 2048 * (half ^ 1) + 16 * lane;
      const f32x4 o0 = *(LAS f32x4*)(lp(scr)), o1 = *(LAS f32x4*)(lp(scr) + 1024);
      SB();
#pragma unroll
      for (int q = 0; q < 12; ++q) rd2(0, q);
      SB();
#pragma unroll
      for (int r = 0; r < 4; ++r) {
        sv[r] = accS[0][0][r] + o0[r];
        sv[4 + r] = accS[1][0][r] + o1[r];
      }
      float m = fmaxf(fmaxf(fmaxf(sv[0], sv[1]), fmaxf(sv[2], sv[3])), fmaxf(fmaxf(sv[4], sv[5]), fmaxf(sv[6], sv[7])));
      const auto s1 = __builtin_amdgcn_permlane16_swap(__float_as_uint(m), __float_as_uint(m), false, false);
      m = fmaxf(__uint_as_float(s1[0]), __uint_as_float(s1[1]));
      const auto s2 = __builtin_amdgcn_permlane32_swap(__float_as_uint(m), __float_as_uint(m), false, false);
      m = fmaxf(__uint_as_float(s2[0]), __uint_as_float(s2[1]));
      mc = -m * cs;
    }
    SB();
    auto smx = [&](int n, int m) {
      if (n == 0) {
        if (m == 0 || m == 1) {
#pragma unroll
          for (int r = 0; r < 4; ++r) sv[4 * m + r] = __builtin_fmaf(sv[4 * m + r], cs, mc);
        } else if (m <= 5) {
          sv[2 * (m - 2)] = __builtin_amdgcn_exp2f(sv[2 * (m - 2)]);
          sv[2 * (m - 2) + 1] = __builtin_amdgcn_exp2f(sv[2 * (m - 2) + 1]);
        } else if (m == 6) {
          t0 = (sv[0] + sv[1]) + (sv[2] + sv[3]);
        } else if (m == 7) {
          t1 = (sv[4] + sv[5]) + (sv[6] + sv[7]);
          sum = t0 + t1;
        } else if (m == 8) {
          const auto s1 = __builtin_amdgcn_permlane16_swap(__float_as_uint(sum), __float_as_uint(sum), false, false);
          sum = __uint_as_float(s1[0]) + __uint_as_float(s1[1]);
        } else {
          const auto s2 = __builtin_amdgcn_permlane32_swap(__float_as_uint(sum), __float_as_uint(sum), false, false);
          sum = __builtin_amdgcn_rcpf(__uint_as_float(s2[0]) + __uint_as_float(s2[1]));
        }
      } else if (n == 1) {
        if (m == 1 || m == 2) {
#pragma unroll
          for (int r = 0; r < 4; ++r) sv[4 * (m - 1) + r] *= sum;
        } else if (m == 3) {
#pragma unroll
          for (int d = 0; d < 4; ++d) pfo[d] = pk2(sv[2 * d], sv[2 * d + 1]);
        } else if (m == 4) {
#pragma unroll
          for (int d = 0; d < 4; ++d) pfo[d] = pfo[d] < 0xfffffffeu ? pfo[d] : 0xfffffffeu;
        } else if (m == 5) {
          __builtin_amdgcn_raw_buffer_store_b128(pfo, rWs, lane * 16 + 1024 * half, WS_P + ((2 * g + b) * 32 + h) * 2048, 16  );
        }
      }
    };
    u32 fval = 0;
    static_for<0, 24>([&](auto nc_) {
      constexpr int n = decltype(nc_)::value;
      constexpr int s = 10 + (n >> 1), ks = n & 1, hi = n / 6;
      constexpr bool last = (n % 6 == 5);
      if (ks == 1 && s < NST - 1) {
        wait_lgkm0();
        bar();
      }
#pragma unroll
      for (int m = 0; m < 10; ++m) {
        mm2(n, m >> 1, m & 1);
        SB();
        if (n + 1 < 24) {
#pragma unroll
          for (int q = 0; q < 12; ++q)
            if (q >= rsched2(ks, m) && q < rsched2(ks, m + 1)) rd2(n + 1, q);
        }
        if (n < 2) smx(n, m);
        if (last) {
          if (m == 4) pack(hi, 0);
          if (m == 6) pack(hi, 1);
          if (m == 8) pack(hi, 2);
          if (m == 9) { pack(hi, 3); pack(hi, 4); }
        } else if (ks == 0 && s < NST - 1 && m == 8) adv2(s + 1);
        SB();
      }
      if (n == PCHK_N - 1) {
        fval = *(LAS u32*)(lp(lbase + KREG + 16384 + 4 * (8 * b + (lane & 7))));
#pragma unroll
        for (int i = 0; i < 8; ++i) Pf[i] = *(LAS v4u*)(lp(lbase + KREG + 16 * lane) + (b * 8 + i) * 1024);
      }
      if (n == PCHK_N) {
        asm volatile("" : "+v"(X[0][0]), "+v"(X[0][1]), "+v"(X[0][2]), "+v"(X[0][3]), "+v"(X[0][4]), "+v"(X[1][0]), "+v"(X[1][1]), "+v"(X[1][2]),
                     "+v"(X[1][3]), "+v"(X[1][4]));
        if (!(__all(fval == e) && all_written(Pf))) {
          poll_eq((GAS u32*)(ws + WS_FLAGP + (((2 * g + b) * 32 + 4 * j) * 2 + (lane & 7)) * 128), e);
          asm volatile("" ::: "memory");
          const u32 pb0 = WS_P + ((2 * g + b) * 32 + 4 * j) * 2048;
          u32 spins = 0;
          do {
            load4x2_sc1(Pf, lane * 16, rWs, pb0 + (hrot & 3) * 2048, pb0 + ((hrot + 1) & 3) * 2048, pb0 + ((hrot + 2) & 3) * 2048,
                        pb0 + ((hrot + 3) & 3) * 2048);
          } while (!all_written(Pf) && ++spins < SPIN_CAP);
        }
      }
#pragma unroll
      for (int k = 0; k < 40; ++k)
        if (k >= pvsched(n) && k < pvsched(n + 1)) pvi(k);
      SB();
    });
    {
      const u32 pos = (r16 >> 1) * 8 + (r16 & 1) * 4 + g4;
      const u32 sb = WS_SLAB + (w * 4 + cw) * 5120;
#pragma unroll
      for (int cb = 0; cb < 5; ++cb) {
        v4u v;
        v[0] = pk2(O[cb][0][0], O[cb][0][1]); v[1] = pk2(O[cb][0][2], O[cb][0][3]);
        v[2] = pk2(O[cb][1][0], O[cb][1][1]); v[3] = pk2(O[cb][1][2], O[cb][1][3]);
        __builtin_amdgcn_raw_buffer_store_b128(v, rWs, pos * 16 + 1024 * cb, sb, 16  );
      }
      wait_vm<0>();
      if (lane == 0) __hip_atomic_store((GAS u32*)(ws + WS_FLAGO + (w * 4 + cw) * 128), e, RLX_AGENT);
    }
    {
      const int w0 = w & ~7;
      const int cb = lane < 40 ? lane >> 3 : 4, rr = lane & 7;
      poll_eq((GAS u32*)(ws + WS_FLAGO + ((w0 + (lane & 7)) * 4 + cw) * 128), e);
      asm volatile("" ::: "memory");
      v4u v[8];
      load8_sc1(v, (8 * j + rr) * 16 + 1024 * cb, rWs, WS_SLAB + (w0 * 4 + cw) * 5120, 4 * 5120);
      if (lane < 40) {
        float a[8];
#pragma unroll
        for (int d = 0; d < 4; ++d) {
          float lo[8], hi[8];
#pragma unroll
          for (int i = 0; i < 8; ++i) {
            lo[i] = __uint_as_float(v[i][d] << 16);
            hi[i] = __uint_as_float(v[i][d] & 0xffff0000u);
          }
          a[2 * d] = ((lo[0] + lo[1]) + (lo[2] + lo[3])) + ((lo[4] + lo[5]) + (lo[6] + lo[7]));
          a[2 * d + 1] = ((hi[0] + hi[1]) + (hi[2] + hi[3])) + ((hi[4] + hi[5]) + (hi[6] + hi[7]));
        }
        const int qp = 2 * j + (rr >> 2), gg = rr & 3;
        const u32 col = 160 * hh + 80 * half + 16 * cb + 4 * gg;
        GAS char* o0 = (GAS char*)out + (size_t)((2 * g + b) * 32 + qp) * 1280 + col * 2;
        *(GAS v2u*)o0 = (v2u){pk2(a[0], a[1]), pk2(a[2], a[3])};
        *(GAS v2u*)(o0 + 16 * 1280) = (v2u){pk2(a[4], a[5]), pk2(a[6], a[7])};
      }
      if (j == 0 && hh == 0 && cw == 0 && lane == 0) __hip_atomic_store((GAS u32*)(ws + WS_E + g * 128), e, RLX_AGENT);
    }
  };

  if (wave < 4) compute(); else loader();
}

extern "C" void kernel_launch(void* const* d_in, const int* in_sizes, int n_in, void* d_out, int out_size, void* d_ws, size_t ws_size,
                              hipStream_t stream) {
  (void)hipFuncSetAttribute((const void*)gqa_block_kernel, hipFuncAttributeMaxDynamicSharedMemorySize, LDS_BYTES);
  gqa_block_kernel<<<256, 512, LDS_BYTES, stream>>>((const char*)d_in[0], (const char*)d_in[1], (const char*)d_in[2], (const char*)d_in[3],
                                                     (const char*)d_in[4], (char*)d_out, (char*)d_ws);
}
```

```cpp
#include <hip/hip_runtime.h>
#include <hip/hip_bf16.h>
#include <stdint.h>

typedef unsigned u32;
typedef u32 v4u __attribute__((ext_vector_type(4)));
typedef u32 v2u __attribute__((ext_vector_type(2)));
typedef float f32x2 __attribute__((ext_vector_type(2)));
typedef float f32x4 __attribute__((ext_vector_type(4)));
typedef short s16x4 __attribute__((ext_vector_type(4)));
typedef __bf16 bf16x2 __attribute__((ext_vector_type(2)));
typedef __bf16 bf16x8 __attribute__((ext_vector_type(8)));
#define LAS __attribute__((address_space(3)))
#define GAS __attribute__((address_space(1)))
#define DI __device__ __forceinline__
#define RLX_AGENT __ATOMIC_RELAXED, __HIP_MEMORY_SCOPE_AGENT
#define SB() __builtin_amdgcn_sched_barrier(0)

namespace {
constexpr int SLOT = 32768, ACT = 24576, NSLOT = 4, KREG = 0, SCR = 24576, RING = 32768, LDS_BYTES = RING + SLOT * NSLOT;
constexpr int NST = 22;
constexpr u32 WS_E = 0, WS_FLAGP = 4096, WS_FLAGO = 135168, WS_P = 266240, WS_SLAB = 1314816;
constexpr u32 WS_USED = WS_SLAB + 256 * 20480;
constexpr u32 SPIN_CAP = 1u << 18;
constexpr int LA = 8;
constexpr int EPO_N = 4;
constexpr int FDMA_S = 10;
constexpr int PDMA_S = 15;
constexpr int PCHK_N = 14;

template <bool SC1>
DI void dma(u32 voff, __amdgpu_buffer_rsrc_t rs, u32 soff, u32 ldsdst) {
  if constexpr (SC1)
    asm volatile("s_mov_b32 m0, %2\n\ts_nop 0\n\tbuffer_load_dwordx4 %0, %1, %3 offen sc1 lds" ::"v"(voff), "s"(rs), "s"(ldsdst), "s"(soff) : "memory");
  else
    asm volatile("s_mov_b32 m0, %2\n\ts_nop 0\n\tbuffer_load_dwordx4 %0, %1, %3 offen lds" ::"v"(voff), "s"(rs), "s"(ldsdst), "s"(soff) : "memory");
}
DI void dma_word(u32 voff, __amdgpu_buffer_rsrc_t rs, u32 soff, u32 ldsdst) {
  asm volatile("s_mov_b32 m0, %2\n\ts_nop 0\n\tbuffer_load_dword %0, %1, %3 offen sc1 lds" ::"v"(voff), "s"(rs), "s"(ldsdst), "s"(soff) : "memory");
}
template <int N> DI void wait_vm() { asm volatile("s_waitcnt vmcnt(%0)" ::"i"(N) : "memory"); }
DI void bar() {
  SB();
  asm volatile("s_barrier" ::: "memory");
  SB();
}
DI void wait_lgkm0() {
  asm volatile("" ::: "memory");
  __builtin_amdgcn_s_waitcnt(0xC07F);
  asm volatile("" ::: "memory");
}
DI u32 rfl(u32 v) { return __builtin_amdgcn_readfirstlane(v); }
DI u32 pk2(float a, float b) {
  f32x2 v = {a, b};
  return __builtin_bit_cast(u32, __builtin_convertvector(v, bf16x2));
}
DI LAS char* lp(u32 a) { return (LAS char*)a; }
DI bf16x8 lds128(u32 a, int off) { return *(LAS bf16x8*)(lp(a) + off); }
DI s16x4 ldstr4(u32 a, int off) { return __builtin_amdgcn_ds_read_tr16_b64_v4i16((LAS s16x4*)(lp(a) + off)); }
DI bf16x8 cat8(s16x4 a, s16x4 b) { return __builtin_bit_cast(bf16x8, __builtin_shufflevector(a, b, 0, 1, 2, 3, 4, 5, 6, 7)); }
DI f32x4 mfma16(bf16x8 a, bf16x8 b, f32x4 c) { return __builtin_amdgcn_mfma_f32_16x16x32_bf16(a, b, c, 0, 0, 0); }

template <int V> struct IC { static constexpr int value = V; };
template <int I, int N, class F> DI void static_for(F&& f) {
  if constexpr (I < N) {
    f(IC<I>{});
    static_for<I + 1, N>(f);
  }
}
constexpr int rsched1(int ks, int m) { return ks == 0 ? (m <= 7 ? 2 * m : 14) : (m == 0 ? 0 : (m <= 4 ? m + 1 : (m <= 12 ? m + 2 : 14))); }
constexpr int rsched2(int ks, int m) { return ks == 0 ? (m <= 4 ? 2 * m : (m <= 8 ? m + 4 : 12)) : (m == 0 ? 0 : (m <= 4 ? m + 1 : m + 2)); }
constexpr int pvsched(int n) {
  if (n <= PCHK_N) return 0;
  if (n <= 18) { const int a = (20 * (n - PCHK_N - 1) + (17 - PCHK_N)) / (17 - PCHK_N); return a > 20 ? 20 : a; }
  if (n <= 23) return 20 + 2 * (n - 18);
  return 40;
}
static_assert(pvsched(18) == 20 && pvsched(23) == 30 && pvsched(24) == 40 && PCHK_N >= 11 && PCHK_N <= 16, "");
static_assert(2 * (PDMA_S + 1 - 10) - 1 <= PCHK_N - 1 && FDMA_S < PDMA_S && FDMA_S >= 10, "flags before fragments, both landed before they are read");

DI void load8_sc1(v4u (&v)[8], u32 vo, __amdgpu_buffer_rsrc_t rs, u32 s0, u32 step) {
  u32 s1 = s0 + step, s2 = s1 + step, s3 = s2 + step, s4 = s3 + step, s5 = s4 + step, s6 = s5 + step, s7 = s6 + step;
  asm volatile(
      "s_nop 4\n\t"
      "buffer_load_dwordx4 %0, %8, %9, %10 offen sc1\n\t"
      "buffer_load_dwordx4 %1, %8, %9, %11 offen sc1\n\t"
      "buffer_load_dwordx4 %2, %8, %9, %12 offen sc1\n\t"
      "buffer_load_dwordx4 %3, %8, %9, %13 offen sc1\n\t"
      "buffer_load_dwordx4 %4, %8, %9, %14 offen sc1\n\t"
      "buffer_load_dwordx4 %5, %8, %9, %15 offen sc1\n\t"
      "buffer_load_dwordx4 %6, %8, %9, %16 offen sc1\n\t"
      "buffer_load_dwordx4 %7, %8, %9, %17 offen sc1\n\t"
      "s_waitcnt vmcnt(0)"
      : "=&v"(v[0]), "=&v"(v[1]), "=&v"(v[2]), "=&v"(v[3]), "=&v"(v[4]), "=&v"(v[5]), "=&v"(v[6]), "=&v"(v[7])
      : "v"(vo), "s"(rs), "s"(s0), "s"(s1), "s"(s2), "s"(s3), "s"(s4), "s"(s5), "s"(s6), "s"(s7)
      : "memory");
}
DI void load4x2_sc1(v4u (&v)[8], u32 vo, __amdgpu_buffer_rsrc_t rs, u32 s0, u32 s1, u32 s2, u32 s3) {
  asm volatile(
      "s_nop 4\n\t"
      "buffer_load_dwordx4 %0, %8, %9, %10 offen sc1\n\t"
      "buffer_load_dwordx4 %1, %8, %9, %10 offen offset:1024 sc1\n\t"
      "buffer_load_dwordx4 %2, %8, %9, %11 offen sc1\n\t"
      "buffer_load_dwordx4 %3, %8, %9, %11 offen offset:1024 sc1\n\t"
      "buffer_load_dwordx4 %4, %8, %9, %12 offen sc1\n\t"
      "buffer_load_dwordx4 %5, %8, %9, %12 offen offset:1024 sc1\n\t"
      "buffer_load_dwordx4 %6, %8, %9, %13 offen sc1\n\t"
      "buffer_load_dwordx4 %7, %8, %9, %13 offen offset:1024 sc1\n\t"
      "s_waitcnt vmcnt(0)"
      : "=&v"(v[0]), "=&v"(v[1]), "=&v"(v[2]), "=&v"(v[3]), "=&v"(v[4]), "=&v"(v[5]), "=&v"(v[6]), "=&v"(v[7])
      : "v"(vo), "s"(rs), "s"(s0), "s"(s1), "s"(s2), "s"(s3)
      : "memory");
}
DI void poll_eq(GAS u32* p, u32 want) {
  for (u32 spins = 0; spins < SPIN_CAP; ++spins) {
    const u32 v = __hip_atomic_load(p, RLX_AGENT);
    if (__all(v == want)) break;
    __builtin_amdgcn_s_sleep(1);
  }
}
DI bool all_written(const v4u (&v)[8]) {
  u32 mx = 0;
#pragma unroll
  for (int i = 0; i < 8; ++i) {
    const u32 a = v[i][0] > v[i][1] ? v[i][0] : v[i][1], c = v[i][2] > v[i][3] ? v[i][2] : v[i][3];
    const u32 d = a > c ? a : c;
    mx = mx > d ? mx : d;
  }
  return __all(mx != 0xffffffffu);
}
}

__global__ __launch_bounds__(512, 2) void gqa_block_kernel(const char* __restrict__ x, const char* __restrict__ kc, const char* __restrict__ vc,
                                                           const char* __restrict__ wq, const char* __restrict__ wo, char* __restrict__ out,
                                                           char* wsp) {
  extern __shared__ __attribute__((aligned(16))) char smem_[];
  const int w = blockIdx.x;
  const int wave = rfl(threadIdx.x >> 6);
  const int lane = threadIdx.x & 63;
  const int j = w & 7, g = w >> 5, hh = (w >> 3) & 3;
  const int h = 4 * j + hh;
  const int hrot = hh + g;
  GAS char* ws = (GAS char*)wsp;
  const u32 lbase = (u32)(size_t)(LAS char*)smem_;

  auto loader = [&]() __attribute__((always_inline)) {
    const int lw = wave - 4;
    __builtin_amdgcn_s_setprio(1);
    const __amdgpu_buffer_rsrc_t rX = __builtin_amdgcn_make_buffer_rsrc((void*)x, 0, 512 * 1280, 0x00020000);
    const __amdgpu_buffer_rsrc_t rWq = __builtin_amdgcn_make_buffer_rsrc((void*)wq, 0, 640 * 12288, 0x00020000);
    u32 ra[2], ca[2], vA[2], vKV[2];
#pragma unroll
    for (int i = 0; i < 2; ++i) {
      ra[i] = 8 * i + (lane >> 3);
      ca[i] = 16 * ((lane & 7) ^ ((ra[i] >> 1) & 7));
      vA[i] = ra[i] * 1280 + ca[i];
    }
    u32 kk[6], cc[6], vW[6];
#pragma unroll
    for (int i = 0; i < 6; ++i) {
      const u32 L = 8 * i + (lane >> 3), k = (L * 43) >> 7;
      kk[i] = k;
      cc[i] = 128 * (L - 3 * k) + 16 * ((lane & 7) ^ ((k & 2) << 1) ^ ((k & 8) >> 2));
      vW[i] = k * 12288 + cc[i];
    }
#pragma unroll
    for (int i = 0; i < 2; ++i) vKV[i] = ra[i] * 384 + ca[i];
    const u32 ldsW = lbase + RING + lw * 6144, ldsA = lbase + RING + ACT + lw * 2048;
    const u32 soW0 = h * 384 + 16 * lw * 12288, soA0 = (64 * g + 16 * lw) * 1280;
    const int bl = 2 * g + (lw >> 1);
    const u32 soKV0 = (u32)(bl * 32 * 32 + 16 * (lw & 1)) * 384;
    const u32 L0 = (5 * hh) >> 1;
    int kcur = (g + 3 * hh) % 10;
    const int crot = g >> 2;
    const int kpos = ((w >> 3) + j) & 7;
    const __amdgpu_buffer_rsrc_t rWs = __builtin_amdgcn_make_buffer_rsrc((void*)wsp, 0, WS_USED, 0x00020000);
    u32 Ev = 0;
    auto after_barrier = [&](int s) {
      if (s == 0) {
        if (lw == 0) dma_word(0, rWs, WS_E + g * 128, lbase + SCR);
        asm volatile("buffer_load_dword %0, %1, %2, %3 offen sc1" : "+v"(Ev) : "v"(0u), "s"(rWs), "s"(WS_E + g * 128) : "memory");
        const v4u ones = {~0u, ~0u, ~0u, ~0u};
        asm volatile("buffer_store_dwordx4 %0, %1, %2, %3 offen sc1\n\ts_nop 1" ::"v"(ones), "v"(lane * 16 + 1024 * (lw & 1)), "s"(rWs),
                     "s"(WS_P + (bl * 32 + h) * 2048)
                     : "memory");
      }
      if (s == 1) {
        asm volatile("" : "+v"(Ev));
        const u32 ev = Ev + 1;
        if (lane == 0)
          asm volatile("buffer_store_dword %0, %1, %2, %3 offen sc1\n\ts_nop 1" ::"v"(ev), "v"(0u), "s"(rWs),
                       "s"(WS_FLAGP + ((bl * 32 + h) * 2 + (lw & 1)) * 128)
                       : "memory");
      }
      if (s == FDMA_S && lw == 0)
        dma_word((((lane >> 3) & 1) * 64 + (lane & 7)) * 128, rWs, WS_FLAGP + ((2 * g * 32 + 4 * j) * 2) * 128, lbase + KREG + 16384);
      if (s == PDMA_S) {
#pragma unroll
        for (int i = 0; i < 2; ++i)
#pragma unroll
          for (int qb = 0; qb < 2; ++qb) {
            const int hx = 2 * (lw & 1) + i;
            dma<true>(lane * 16, rWs, WS_P + (bl * 32 + 4 * j + ((hrot + hx) & 3)) * 2048 + 1024 * qb,
                      lbase + KREG + (((lw >> 1) * 4 + hx) * 2 + qb) * 1024);
          }
      }
      if (s < 8 && s == kpos) {
        const __amdgpu_buffer_rsrc_t rK = __builtin_amdgcn_make_buffer_rsrc((void*)kc, 0, 512 * 12288, 0x00020000);
#pragma unroll
        for (int c = 0; c < 3; ++c)
#pragma unroll
          for (int i = 0; i < 2; ++i) dma<false>(vKV[i], rK, soKV0 + h * 12288 + 128 * c, lbase + KREG + 8192 * c + lw * 2048 + 1024 * i);
      }
    };
    static_for<0, NST>([&](auto tc_) {
      constexpr int t = decltype(tc_)::value;
      constexpr u32 slot = (t % NSLOT) * SLOT;
      u32 soW, soA;
      if constexpr (t < 10) {
        soW = soW0 + kcur * (64 * 12288);
        soA = soA0 + kcur * 128;
        kcur = kcur == 9 ? 0 : kcur + 1;
      } else {
        if constexpr (t == 10) {
#pragma unroll
          for (int i = 0; i < 2; ++i) vA[i] = vKV[i];
#pragma unroll
          for (int i = 0; i < 6; ++i) vW[i] = kk[i] * 1280 + cc[i];
        }
        constexpr int hi = (t - 10) / 3, c0 = (t - 10) % 3;
        const int c = c0 + crot >= 3 ? c0 + crot - 3 : c0 + crot;
        const u32 hp = 4 * j + ((hrot + hi) & 3);
        soW = (hp * 192 + 64 * c + 16 * lw) * 1280 + 128 * L0;
        soA = soKV0 + hp * 12288 + 128 * c;
      }
      static_for<0, 8>([&](auto ic_) {
        constexpr int i = decltype(ic_)::value, p = 8 * t + i;
        if constexpr (t < 10) {
          if constexpr (i < 2) dma<false>(vA[i], rX, soA, slot + ldsA + 1024 * i);
          else dma<false>(vW[i - 2], rWq, soW, slot + ldsW + 1024 * (i - 2));
        } else {
          const __amdgpu_buffer_rsrc_t rV = __builtin_amdgcn_make_buffer_rsrc((void*)vc, 0, 512 * 12288, 0x00020000);
          const __amdgpu_buffer_rsrc_t rWo = __builtin_amdgcn_make_buffer_rsrc((void*)wo, 0, 6144 * 1280, 0x00020000);
          if constexpr (i < 2) dma<false>(vA[i], rV, soA, slot + ldsA + 1024 * i);
          else dma<false>(vW[i - 2], rWo, soW, slot + ldsW + 1024 * (i - 2));
        }
        if constexpr (p + 1 - LA >= 8 && (p + 1 - LA) % 8 == 0) {
          constexpr int s = (p + 1 - LA) / 8 - 1;
          wait_vm<LA>();
          bar();
          if constexpr (s < 8 || s == FDMA_S || s == PDMA_S) after_barrier(s);
        }
      });
    });
    static_for<(8 * NST - LA) / 8, NST>([&](auto sc_) {
      constexpr int s = decltype(sc_)::value;
      wait_vm<8 * (NST - 1 - s)>();
      bar();
    });
  };

  auto compute = [&]() __attribute__((always_inline)) {
    const int cw = wave, b = cw >> 1, half = cw & 1;
    const __amdgpu_buffer_rsrc_t rWs = __builtin_amdgcn_make_buffer_rsrc((void*)wsp, 0, WS_USED, 0x00020000);
    const int r16 = lane & 15, g4 = lane >> 4, q4 = (lane & 15) >> 2, p4 = lane & 3;
    const int x2 = (g4 & 1) | ((q4 >> 1) << 1);
    const u32 fsw = (r16 >> 1) & 7;
    u32 e = 0, Ev = 0;
    f32x4 accS[2][2];
    {
      f32x4 accQ[6][2];
      s16x4 kl[3][2], kh[3][2];
      u32 aW[6], aX[2], aY[2], aK[3][2];
#pragma unroll
      for (int tb = 0; tb < 6; ++tb) aW[tb] = lbase + RING + 384 * (8 * g4 + q4) + 32 * ((6 * half + tb) ^ x2) + 16 * (p4 >> 1) + 8 * (p4 & 1);
#pragma unroll
      for (int ks = 0; ks < 2; ++ks) {
        aX[ks] = lbase + RING + ACT + 128 * (32 * b + 16 * half + r16) + 16 * ((4 * ks + g4) ^ fsw);
        aY[ks] = lbase + RING + ACT + 128 * (32 * b + 16 * (half ^ 1) + r16) + 16 * ((4 * ks + g4) ^ fsw);
      }
#pragma unroll
      for (int u = 0; u < 3; ++u)
#pragma unroll
        for (int a = 0; a < 2; ++a) {
          const int ch = 12 * half + 4 * u + 2 * a + (g4 >> 1);
          aK[u][a] = lbase + KREG + 8192 * (ch >> 3) + 128 * (32 * b + r16) + 16 * ((ch & 7) ^ fsw) + 8 * (g4 & 1);
        }
#pragma unroll
      for (int i = 0; i < 6; ++i) asm volatile("" : "+v"(aW[i]));
#pragma unroll
      for (int i = 0; i < 2; ++i) asm volatile("" : "+v"(aX[i]), "+v"(aY[i]));
#pragma unroll
      for (int i = 0; i < 6; ++i) asm volatile("" : "+v"(aK[i >> 1][i & 1]));

      s16x4 wl[2][6], wh[2][6];
      bf16x8 xf[2][2];
      auto rd1 = [&](int n, int part) {
        const int set = n & 1, ks = n & 1;
        if (part == 0) xf[set][0] = lds128(aX[ks], 0);
        else if (part == 3) xf[set][1] = lds128(aY[ks], 0);
        else {
          const int q = part < 3 ? part - 1 : part - 2, tb = q >> 1;
          if (q & 1) wh[set][tb] = ldstr4(aW[tb], 384 * (32 * ks + 4));
          else wl[set][tb] = ldstr4(aW[tb], 384 * 32 * ks);
        }
      };
      auto rdk = [&](int m) {
        const int u = m >> 2, kb = (m >> 1) & 1, a = m & 1;
        const s16x4 v = *(LAS s16x4*)(lp(aK[u][a]) + 2048 * kb);
        if (a) kh[u][kb] = v; else kl[u][kb] = v;
      };
      auto adv1 = [&](int s) {
        const u32 d = (s % NSLOT == 0) ? (u32)(-(NSLOT - 1) * SLOT) : (u32)SLOT;
#pragma unroll
        for (int i = 0; i < 6; ++i) { aW[i] += d; asm volatile("" : "+v"(aW[i])); }
#pragma unroll
        for (int i = 0; i < 2; ++i) { aX[i] += d; aY[i] += d; asm volatile("" : "+v"(aX[i]), "+v"(aY[i])); }
      };
      auto mm1 = [&](int n, int tb, int qb) {
        const int set = n & 1;
        const f32x4 c0 = (n == 0) ? (f32x4){0.f, 0.f, 0.f, 0.f} : accQ[tb][qb];
        accQ[tb][qb] = mfma16(cat8(wl[set][tb], wh[set][tb]), xf[set][qb], c0);
      };
      bar();
#pragma unroll
      for (int q = 0; q < 14; ++q) rd1(0, q);
      SB();
#pragma unroll
      for (int n = 0; n < 19; ++n) {
        const int s = n >> 1, ks = n & 1;
        if (ks == 1) {
          wait_lgkm0();
          bar();
        }
#pragma unroll
        for (int m = 0; m < 12; ++m) {
          mm1(n, m >> 1, m & 1);
          SB();
#pragma unroll
          for (int q = 0; q < 14; ++q)
            if (q >= rsched1(ks, m) && q < rsched1(ks, m + 1)) rd1(n + 1, q);
          if (ks == 0 && s < 9 && m == 9) adv1(s + 1);
          SB();
        }
        if (n == EPO_N - 2) Ev = *(LAS u32*)(lp(lbase + SCR));
        if (n == EPO_N) {
          e = rfl(Ev) + 1;
          asm volatile("" : "+s"(e));
        }
        SB();
      }
      v4u qp[2];
      auto qpack = [&](int u, int qb) {
        qp[qb][0] = pk2(accQ[2 * u][qb][0], accQ[2 * u][qb][1]);
        qp[qb][1] = pk2(accQ[2 * u][qb][2], accQ[2 * u][qb][3]);
        qp[qb][2] = pk2(accQ[2 * u + 1][qb][0], accQ[2 * u + 1][qb][1]);
        qp[qb][3] = pk2(accQ[2 * u + 1][qb][2], accQ[2 * u + 1][qb][3]);
      };
      auto smm = [&](int u) {
#pragma unroll
        for (int qb = 1; qb >= 0; --qb)
#pragma unroll
          for (int kb = 0; kb < 2; ++kb) {
            const f32x4 c0 = u == 0 ? (f32x4){0.f, 0.f, 0.f, 0.f} : accS[kb][qb];
            accS[kb][qb] = mfma16(cat8(kl[u][kb], kh[u][kb]), __builtin_bit_cast(bf16x8, qp[qb]), c0);
          }
      };
#pragma unroll
      for (int m = 0; m < 12; ++m) {
        mm1(19, m >> 1, m & 1);
        SB();
        if (m < 6) { rdk(2 * m); rdk(2 * m + 1); }
        if (m == 5) qpack(0, 0);
        if (m == 6) qpack(0, 1);
        if (m == 7) smm(0);
        if (m == 9) qpack(1, 0);
        if (m == 10) qpack(1, 1);
        if (m == 11) smm(1);
        SB();
      }
      qpack(2, 0);
      qpack(2, 1);
      smm(2);
      const u32 scw = lbase + SCR + 4096 * b + 2048 * half + 16 * lane;
#pragma unroll
      for (int kb = 0; kb < 2; ++kb) *(LAS f32x4*)(lp(scw) + 1024 * kb) = accS[kb][1];
      wait_lgkm0();
    }
    const int c016 = (hh & 1) * 2;
    u32 aW[5], aV[2];
#pragma unroll
    for (int cb = 0; cb < 5; ++cb)
      aW[cb] = lbase + RING + SLOT * (10 % NSLOT) + 384 * (8 * g4 + q4) + 32 * ((c016 + 5 * half + cb) ^ x2) + 16 * (p4 >> 1) + 8 * (p4 & 1);
#pragma unroll
    for (int ks = 0; ks < 2; ++ks) aV[ks] = lbase + RING + SLOT * (10 % NSLOT) + ACT + 128 * (32 * b + r16) + 16 * ((4 * ks + g4) ^ fsw);
#pragma unroll
    for (int i = 0; i < 5; ++i) asm volatile("" : "+v"(aW[i]));
#pragma unroll
    for (int i = 0; i < 2; ++i) asm volatile("" : "+v"(aV[i]));
    bar();
    s16x4 wl[2][5], wh[2][5];
    bf16x8 vf[2][2];
    auto rd2 = [&](int n, int part) {
      const int set = n & 1, ks = n & 1;
      if (part == 0) vf[set][0] = lds128(aV[ks], 0);
      else if (part == 3) vf[set][1] = lds128(aV[ks], 2048);
      else {
        const int q = part < 3 ? part - 1 : part - 2, cb = q >> 1;
        if (q & 1) wh[set][cb] = ldstr4(aW[cb], 384 * (32 * ks + 4));
        else wl[set][cb] = ldstr4(aW[cb], 384 * 32 * ks);
      }
    };
    auto adv2 = [&](int s) {
      const u32 d = (s % NSLOT == 0) ? (u32)(-(NSLOT - 1) * SLOT) : (u32)SLOT;
#pragma unroll
      for (int i = 0; i < 5; ++i) { aW[i] += d; asm volatile("" : "+v"(aW[i])); }
#pragma unroll
      for (int i = 0; i < 2; ++i) { aV[i] += d; asm volatile("" : "+v"(aV[i])); }
    };
    f32x4 X[2][5];
    v4u Xp[4][5];
    v4u Pf[8];
    f32x4 O[5][2];
#pragma unroll
    for (int cb = 0; cb < 5; ++cb)
#pragma unroll
      for (int qb = 0; qb < 2; ++qb) O[cb][qb] = (f32x4){0.f, 0.f, 0.f, 0.f};
    auto mm2 = [&](int n, int cb, int rb) {
      const int set = n & 1;
      const f32x4 c0 = (n % 6 == 0) ? (f32x4){0.f, 0.f, 0.f, 0.f} : X[rb][cb];
      X[rb][cb] = mfma16(vf[set][rb], cat8(wl[set][cb], wh[set][cb]), c0);
    };
    auto pack = [&](int hi, int cb) {
      Xp[hi][cb][0] = pk2(X[0][cb][0], X[0][cb][1]);
      Xp[hi][cb][1] = pk2(X[0][cb][2], X[0][cb][3]);
      Xp[hi][cb][2] = pk2(X[1][cb][0], X[1][cb][1]);
      Xp[hi][cb][3] = pk2(X[1][cb][2], X[1][cb][3]);
      asm volatile("" : "+v"(Xp[hi][cb]));
    };
    auto pvi = [&](int k) {
      const int hi = k / 10, cb = (k % 10) >> 1, qb = k & 1;
      O[cb][qb] = mfma16(__builtin_bit_cast(bf16x8, Xp[hi][cb]), __builtin_bit_cast(bf16x8, Pf[2 * hi + qb]), O[cb][qb]);
    };
    float sv[8], mc = 0.f, sum = 0.f, t0 = 0.f, t1 = 0.f;
    v4u pfo;
    constexpr float scale = 0.07216878364870322f, cs = 1.4426950408889634f;
    {
      const u32 scr = lbase + SCR + 4096 * b + 2048 * (half ^ 1) + 16 * lane;
      const f32x4 o0 = *(LAS f32x4*)(lp(scr)), o1 = *(LAS f32x4*)(lp(scr) + 1024);
      SB();
#pragma unroll
      for (int q = 0; q < 12; ++q) rd2(0, q);
      SB();
#pragma unroll
      for (int r = 0; r < 4; ++r) {
        sv[r] = accS[0][0][r] + o0[r];
        sv[4 + r] = accS[1][0][r] + o1[r];
      }
#pragma unroll
      for (int d = 0; d < 4; ++d) {
        const u32 a = pk2(sv[2 * d], sv[2 * d + 1]);
        const u32 c = pk2(__uint_as_float(a << 16) * scale, __uint_as_float(a & 0xffff0000u) * scale);
        sv[2 * d] = __uint_as_float(c << 16);
        sv[2 * d + 1] = __uint_as_float(c & 0xffff0000u);
      }
      float m = fmaxf(fmaxf(fmaxf(sv[0], sv[1]), fmaxf(sv[2], sv[3])), fmaxf(fmaxf(sv[4], sv[5]), fmaxf(sv[6], sv[7])));
      const auto s1 = __builtin_amdgcn_permlane16_swap(__float_as_uint(m), __float_as_uint(m), false, false);
      m = fmaxf(__uint_as_float(s1[0]), __uint_as_float(s1[1]));
      const auto s2 = __builtin_amdgcn_permlane32_swap(__float_as_uint(m), __float_as_uint(m), false, false);
      m = fmaxf(__uint_as_float(s2[0]), __uint_as_float(s2[1]));
      mc = -m * cs;
    }
    SB();
    auto smx = [&](int n, int m) {
      if (n == 0) {
        if (m == 0 || m == 1) {
#pragma unroll
          for (int r = 0; r < 4; ++r) sv[4 * m + r] = __builtin_fmaf(sv[4 * m + r], cs, mc);
        } else if (m <= 5) {
          sv[2 * (m - 2)] = __builtin_amdgcn_exp2f(sv[2 * (m - 2)]);
          sv[2 * (m - 2) + 1] = __builtin_amdgcn_exp2f(sv[2 * (m - 2) + 1]);
        } else if (m == 6) {
          t0 = (sv[0] + sv[1]) + (sv[2] + sv[3]);
        } else if (m == 7) {
          t1 = (sv[4] + sv[5]) + (sv[6] + sv[7]);
          sum = t0 + t1;
        } else if (m == 8) {
          const auto s1 = __builtin_amdgcn_permlane16_swap(__float_as_uint(sum), __float_as_uint(sum), false, false);
          sum = __uint_as_float(s1[0]) + __uint_as_float(s1[1]);
        } else {
          const auto s2 = __builtin_amdgcn_permlane32_swap(__float_as_uint(sum), __float_as_uint(sum), false, false);
          sum = __builtin_amdgcn_rcpf(__uint_as_float(s2[0]) + __uint_as_float(s2[1]));
        }
      } else if (n == 1) {
        if (m == 1 || m == 2) {
#pragma unroll
          for (int r = 0; r < 4; ++r) sv[4 * (m - 1) + r] *= sum;
        } else if (m == 3) {
#pragma unroll
          for (int d = 0; d < 4; ++d) pfo[d] = pk2(sv[2 * d], sv[2 * d + 1]);
        } else if (m == 4) {
#pragma unroll
          for (int d = 0; d < 4; ++d) pfo[d] = pfo[d] < 0xfffffffeu ? pfo[d] : 0xfffffffeu;
        } else if (m == 5) {
          __builtin_amdgcn_raw_buffer_store_b128(pfo, rWs, lane * 16 + 1024 * half, WS_P + ((2 * g + b) * 32 + h) * 2048, 16  );
        }
      }
    };
    u32 fval = 0;
    static_for<0, 24>([&](auto nc_) {
      constexpr int n = decltype(nc_)::value;
      constexpr int s = 10 + (n >> 1), ks = n & 1, hi = n / 6;
      constexpr bool last = (n % 6 == 5);
      if (ks == 1 && s < NST - 1) {
        wait_lgkm0();
        bar();
      }
#pragma unroll
      for (int m = 0; m < 10; ++m) {
        mm2(n, m >> 1, m & 1);
        SB();
        if (n + 1 < 24) {
#pragma unroll
          for (int q = 0; q < 12; ++q)
            if (q >= rsched2(ks, m) && q < rsched2(ks, m + 1)) rd2(n + 1, q);
        }
        if (n < 2) smx(n, m);
        if (last) {
          if (m == 4) pack(hi, 0);
          if (m == 6) pack(hi, 1);
          if (m == 8) pack(hi, 2);
          if (m == 9) { pack(hi, 3); pack(hi, 4); }
        } else if (ks == 0 && s < NST - 1 && m == 8) adv2(s + 1);
        SB();
      }
      if (n == PCHK_N - 1) {
        fval = *(LAS u32*)(lp(lbase + KREG + 16384 + 4 * (8 * b + (lane & 7))));
#pragma unroll
        for (int i = 0; i < 8; ++i) Pf[i] = *(LAS v4u*)(lp(lbase + KREG + 16 * lane) + (b * 8 + i) * 1024);
      }
      if (n == PCHK_N) {
        asm volatile("" : "+v"(X[0][0]), "+v"(X[0][1]), "+v"(X[0][2]), "+v"(X[0][3]), "+v"(X[0][4]), "+v"(X[1][0]), "+v"(X[1][1]), "+v"(X[1][2]),
                     "+v"(X[1][3]), "+v"(X[1][4]));
        if (!(__all(fval == e) && all_written(Pf))) {
          poll_eq((GAS u32*)(ws + WS_FLAGP + (((2 * g + b) * 32 + 4 * j) * 2 + (lane & 7)) * 128), e);
          asm volatile("" ::: "memory");
          const u32 pb0 = WS_P + ((2 * g + b) * 32 + 4 * j) * 2048;
          u32 spins = 0;
          do {
            load4x2_sc1(Pf, lane * 16, rWs, pb0 + (hrot & 3) * 2048, pb0 + ((hrot + 1) & 3) * 2048, pb0 + ((hrot + 2) & 3) * 2048,
                        pb0 + ((hrot + 3) & 3) * 2048);
          } while (!all_written(Pf) && ++spins < SPIN_CAP);
        }
      }
#pragma unroll
      for (int k = 0; k < 40; ++k)
        if (k >= pvsched(n) && k < pvsched(n + 1)) pvi(k);
      SB();
    });
    {
      const u32 pos = (r16 >> 1) * 8 + (r16 & 1) * 4 + g4;
      const u32 sb = WS_SLAB + (w * 4 + cw) * 5120;
#pragma unroll
      for (int cb = 0; cb < 5; ++cb) {
        v4u v;
        v[0] = pk2(O[cb][0][0], O[cb][0][1]); v[1] = pk2(O[cb][0][2], O[cb][0][3]);
        v[2] = pk2(O[cb][1][0], O[cb][1][1]); v[3] = pk2(O[cb][1][2], O[cb][1][3]);
        __builtin_amdgcn_raw_buffer_store_b128(v, rWs, pos * 16 + 1024 * cb, sb, 16  );
      }
      wait_vm<0>();
      if (lane == 0) __hip_atomic_store((GAS u32*)(ws + WS_FLAGO + (w * 4 + cw) * 128), e, RLX_AGENT);
    }
    {
      const int w0 = w & ~7;
      const int cb = lane < 40 ? lane >> 3 : 4, rr = lane & 7;
      poll_eq((GAS u32*)(ws + WS_FLAGO + ((w0 + (lane & 7)) * 4 + cw) * 128), e);
      asm volatile("" ::: "memory");
      v4u v[8];
      load8_sc1(v, (8 * j + rr) * 16 + 1024 * cb, rWs, WS_SLAB + (w0 * 4 + cw) * 5120, 4 * 5120);
      if (lane < 40) {
        float a[8];
#pragma unroll
        for (int d = 0; d < 4; ++d) {
          float lo[8], hi[8];
#pragma unroll
          for (int i = 0; i < 8; ++i) {
            lo[i] = __uint_as_float(v[i][d] << 16);
            hi[i] = __uint_as_float(v[i][d] & 0xffff0000u);
          }
          a[2 * d] = ((lo[0] + lo[1]) + (lo[2] + lo[3])) + ((lo[4] + lo[5]) + (lo[6] + lo[7]));
          a[2 * d + 1] = ((hi[0] + hi[1]) + (hi[2] + hi[3])) + ((hi[4] + hi[5]) + (hi[6] + hi[7]));
        }
        const int qp = 2 * j + (rr >> 2), gg = rr & 3;
        const u32 col = 160 * hh + 80 * half + 16 * cb + 4 * gg;
        GAS char* o0 = (GAS char*)out + (size_t)((2 * g + b) * 32 + qp) * 1280 + col * 2;
        *(GAS v2u*)o0 = (v2u){pk2(a[0], a[1]), pk2(a[2], a[3])};
        *(GAS v2u*)(o0 + 16 * 1280) = (v2u){pk2(a[4], a[5]), pk2(a[6], a[7])};
      }
      if (j == 0 && hh == 0 && cw == 0 && lane == 0) __hip_atomic_store((GAS u32*)(ws + WS_E + g * 128), e, RLX_AGENT);
    }
  };

  if (wave < 4) compute(); else loader();
}

extern "C" void kernel_launch(void* const* d_in, const int* in_sizes, int n_in, void* d_out, int out_size, void* d_ws, size_t ws_size,
                              hipStream_t stream) {
  (void)hipFuncSetAttribute((const void*)gqa_block_kernel, hipFuncAttributeMaxDynamicSharedMemorySize, LDS_BYTES);
  gqa_block_kernel<<<256, 512, LDS_BYTES, stream>>>((const char*)d_in[0], (const char*)d_in[1], (const char*)d_in[2], (const char*)d_in[3],
                                                     (const char*)d_in[4], (char*)d_out, (char*)d_ws);
}
```

```cpp
#include <hip/hip_runtime.h>
#include <hip/hip_bf16.h>
#include <stdint.h>

typedef unsigned u32;
typedef u32 v4u __attribute__((ext_vector_type(4)));
typedef u32 v2u __attribute__((ext_vector_type(2)));
typedef float f32x2 __attribute__((ext_vector_type(2)));
typedef float f32x4 __attribute__((ext_vector_type(4)));
typedef short s16x4 __attribute__((ext_vector_type(4)));
typedef __bf16 bf16x2 __attribute__((ext_vector_type(2)));
typedef __bf16 bf16x8 __attribute__((ext_vector_type(8)));
#define LAS __attribute__((address_space(3)))
#define GAS __attribute__((address_space(1)))
#define DI __device__ __forceinline__
#define RLX_AGENT __ATOMIC_RELAXED, __HIP_MEMORY_SCOPE_AGENT
#define SB() __builtin_amdgcn_sched_barrier(0)

namespace {
constexpr int SLOT = 32768, ACT = 24576, NSLOT = 4, KREG = 0, SCR = 24576, RING = 32768, LDS_BYTES = RING + SLOT * NSLOT;
constexpr int NST = 22;
constexpr u32 WS_E = 0, WS_FLAGP = 4096, WS_FLAGO = 135168, WS_P = 266240, WS_SLAB = 1314816;
constexpr u32 WS_USED = WS_SLAB + 256 * 20480;
constexpr u32 SPIN_CAP = 1u << 18;
constexpr int LA = 8;
constexpr int EPO_N = 4;
constexpr int FDMA_S = 10;
constexpr int PDMA_S = 15;
constexpr int PCHK_N = 14;

template <bool SC1>
DI void dma(u32 voff, __amdgpu_buffer_rsrc_t rs, u32 soff, u32 ldsdst) {
  if constexpr (SC1)
    asm volatile("s_mov_b32 m0, %2\n\ts_nop 0\n\tbuffer_load_dwordx4 %0, %1, %3 offen sc1 lds" ::"v"(voff), "s"(rs), "s"(ldsdst), "s"(soff) : "memory");
  else
    asm volatile("s_mov_b32 m0, %2\n\ts_nop 0\n\tbuffer_load_dwordx4 %0, %1, %3 offen lds" ::"v"(voff), "s"(rs), "s"(ldsdst), "s"(soff) : "memory");
}
DI void dma_word(u32 voff, __amdgpu_buffer_rsrc_t rs, u32 soff, u32 ldsdst) {
  asm volatile("s_mov_b32 m0, %2\n\ts_nop 0\n\tbuffer_load_dword %0, %1, %3 offen sc1 lds" ::"v"(voff), "s"(rs), "s"(ldsdst), "s"(soff) : "memory");
}
template <int N> DI void wait_vm() { asm volatile("s_waitcnt vmcnt(%0)" ::"i"(N) : "memory"); }
DI void bar() {
  SB();
  asm volatile("s_barrier" ::: "memory");
  SB();
}
DI void wait_lgkm0() {
  asm volatile("" ::: "memory");
  __builtin_amdgcn_s_waitcnt(0xC07F);
  asm volatile("" ::: "memory");
}
DI u32 rfl(u32 v) { return __builtin_amdgcn_readfirstlane(v); }
DI u32 pk2(float a, float b) {
  f32x2 v = {a, b};
  return __builtin_bit_cast(u32, __builtin_convertvector(v, bf16x2));
}
DI LAS char* lp(u32 a) { return (LAS char*)a; }
DI bf16x8 lds128(u32 a, int off) { return *(LAS bf16x8*)(lp(a) + off); }
DI s16x4 ldstr4(u32 a, int off) { return __builtin_amdgcn_ds_read_tr16_b64_v4i16((LAS s16x4*)(lp(a) + off)); }
DI bf16x8 cat8(s16x4 a, s16x4 b) { return __builtin_bit_cast(bf16x8, __builtin_shufflevector(a, b, 0, 1, 2, 3, 4, 5, 6, 7)); }
DI f32x4 mfma16(bf16x8 a, bf16x8 b, f32x4 c) { return __builtin_amdgcn_mfma_f32_16x16x32_bf16(a, b, c, 0, 0, 0); }

template <int V> struct IC { static constexpr int value = V; };
template <int I, int N, class F> DI void static_for(F&& f) {
  if constexpr (I < N) {
    f(IC<I>{});
    static_for<I + 1, N>(f);
  }
}
constexpr int rsched1(int ks, int m) { return ks == 0 ? (m <= 7 ? 2 * m : 14) : (m == 0 ? 0 : (m <= 4 ? m + 1 : (m <= 12 ? m + 2 : 14))); }
constexpr int rsched2(int ks, int m) { return ks == 0 ? (m <= 4 ? 2 * m : (m <= 8 ? m + 4 : 12)) : (m == 0 ? 0 : (m <= 4 ? m + 1 : m + 2)); }
constexpr int pvsched(int n) {
  if (n <= PCHK_N) return 0;
  if (n <= 18) { const int a = (20 * (n - PCHK_N - 1) + (17 - PCHK_N)) / (17 - PCHK_N); return a > 20 ? 20 : a; }
  if (n <= 23) return 20 + 2 * (n - 18);
  return 40;
}
static_assert(pvsched(18) == 20 && pvsched(23) == 30 && pvsched(24) == 40 && PCHK_N >= 11 && PCHK_N <= 16, "");
static_assert(2 * (PDMA_S + 1 - 10) - 1 <= PCHK_N - 1 && FDMA_S < PDMA_S && FDMA_S >= 10, "flags before fragments, both landed before they are read");

DI void load8_sc1(v4u (&v)[8], u32 vo, __amdgpu_buffer_rsrc_t rs, u32 s0, u32 step) {
  u32 s1 = s0 + step, s2 = s1 + step, s3 = s2 + step, s4 = s3 + step, s5 = s4 + step, s6 = s5 + step, s7 = s6 + step;
  asm volatile(
      "s_nop 4\n\t"
      "buffer_load_dwordx4 %0, %8, %9, %10 offen sc1\n\t"
      "buffer_load_dwordx4 %1, %8, %9, %11 offen sc1\n\t"
      "buffer_load_dwordx4 %2, %8, %9, %12 offen sc1\n\t"
      "buffer_load_dwordx4 %3, %8, %9, %13 offen sc1\n\t"
      "buffer_load_dwordx4 %4, %8, %9, %14 offen sc1\n\t"
      "buffer_load_dwordx4 %5, %8, %9, %15 offen sc1\n\t"
      "buffer_load_dwordx4 %6, %8, %9, %16 offen sc1\n\t"
      "buffer_load_dwordx4 %7, %8, %9, %17 offen sc1\n\t"
      "s_waitcnt vmcnt(0)"
      : "=&v"(v[0]), "=&v"(v[1]), "=&v"(v[2]), "=&v"(v[3]), "=&v"(v[4]), "=&v"(v[5]), "=&v"(v[6]), "=&v"(v[7])
      : "v"(vo), "s"(rs), "s"(s0), "s"(s1), "s"(s2), "s"(s3), "s"(s4), "s"(s5), "s"(s6), "s"(s7)
      : "memory");
}
DI void load4x2_sc1(v4u (&v)[8], u32 vo, __amdgpu_buffer_rsrc_t rs, u32 s0, u32 s1, u32 s2, u32 s3) {
  asm volatile(
      "s_nop 4\n\t"
      "buffer_load_dwordx4 %0, %8, %9, %10 offen sc1\n\t"
      "buffer_load_dwordx4 %1, %8, %9, %10 offen offset:1024 sc1\n\t"
      "buffer_load_dwordx4 %2, %8, %9, %11 offen sc1\n\t"
      "buffer_load_dwordx4 %3, %8, %9, %11 offen offset:1024 sc1\n\t"
      "buffer_load_dwordx4 %4, %8, %9, %12 offen sc1\n\t"
      "buffer_load_dwordx4 %5, %8, %9, %12 offen offset:1024 sc1\n\t"
      "buffer_load_dwordx4 %6, %8, %9, %13 offen sc1\n\t"
      "buffer_load_dwordx4 %7, %8, %9, %13 offen offset:1024 sc1\n\t"
      "s_waitcnt vmcnt(0)"
      : "=&v"(v[0]), "=&v"(v[1]), "=&v"(v[2]), "=&v"(v[3]), "=&v"(v[4]), "=&v"(v[5]), "=&v"(v[6]), "=&v"(v[7])
      : "v"(vo), "s"(rs), "s"(s0), "s"(s1), "s"(s2), "s"(s3)
      : "memory");
}
DI void poll_eq(GAS u32* p, u32 want) {
  for (u32 spins = 0; spins < SPIN_CAP; ++spins) {
    const u32 v = __hip_atomic_load(p, RLX_AGENT);
    if (__all(v == want)) break;
    __builtin_amdgcn_s_sleep(1);
  }
}
DI bool all_written(const v4u (&v)[8]) {
  u32 mx = 0;
#pragma unroll
  for (int i = 0; i < 8; ++i) {
    const u32 a = v[i][0] > v[i][1] ? v[i][0] : v[i][1], c = v[i][2] > v[i][3] ? v[i][2] : v[i][3];
    const u32 d = a > c ? a : c;
    mx = mx > d ? mx : d;
  }
  return __all(mx != 0xffffffffu);
}
}

__global__ __launch_bounds__(512, 2) void gqa_block_kernel(const char* __restrict__ x, const char* __restrict__ kc, const char* __restrict__ vc,
                                                           const char* __restrict__ wq, const char* __restrict__ wo, char* __restrict__ out,
                                                           char* wsp) {
  extern __shared__ __attribute__((aligned(16))) char smem_[];
  const int w = blockIdx.x;
  const int wave = rfl(threadIdx.x >> 6);
  const int lane = threadIdx.x & 63;
  const int j = w & 7, g = w >> 5, hh = (w >> 3) & 3;
  const int h = 4 * j + hh;
  const int hrot = hh + g;
  GAS char* ws = (GAS char*)wsp;
  const u32 lbase = (u32)(size_t)(LAS char*)smem_;

  {
    const unsigned long long tstart = __builtin_amdgcn_s_memrealtime();
    if (threadIdx.x == 0) __hip_atomic_store((GAS unsigned long long*)(ws + (20u << 20) + w * 8), tstart, RLX_AGENT);
  }
  auto loader = [&]() __attribute__((always_inline)) {
    const int lw = wave - 4;
    __builtin_amdgcn_s_setprio(1);
    const __amdgpu_buffer_rsrc_t rX = __builtin_amdgcn_make_buffer_rsrc((void*)x, 0, 512 * 1280, 0x00020000);
    const __amdgpu_buffer_rsrc_t rWq = __builtin_amdgcn_make_buffer_rsrc((void*)wq, 0, 640 * 12288, 0x00020000);
    u32 ra[2], ca[2], vA[2], vKV[2];
#pragma unroll
    for (int i = 0; i < 2; ++i) {
      ra[i] = 8 * i + (lane >> 3);
      ca[i] = 16 * ((lane & 7) ^ ((ra[i] >> 1) & 7));
      vA[i] = ra[i] * 1280 + ca[i];
    }
    u32 kk[6], cc[6], vW[6];
#pragma unroll
    for (int i = 0; i < 6; ++i) {
      const u32 L = 8 * i + (lane >> 3), k = (L * 43) >> 7;
      kk[i] = k;
      cc[i] = 128 * (L - 3 * k) + 16 * ((lane & 7) ^ ((k & 2) << 1) ^ ((k & 8) >> 2));
      vW[i] = k * 12288 + cc[i];
    }
#pragma unroll
    for (int i = 0; i < 2; ++i) vKV[i] = ra[i] * 384 + ca[i];
    const u32 ldsW = lbase + RING + lw * 6144, ldsA = lbase + RING + ACT + lw * 2048;
    const u32 soW0 = h * 384 + 16 * lw * 12288, soA0 = (64 * g + 16 * lw) * 1280;
    const int bl = 2 * g + (lw >> 1);
    const u32 soKV0 = (u32)(bl * 32 * 32 + 16 * (lw & 1)) * 384;
    const u32 L0 = (5 * hh) >> 1;
    int kcur = (g + 3 * hh) % 10;
    const int crot = g >> 2;
    const int kpos = ((w >> 3) + j) & 7;
    const __amdgpu_buffer_rsrc_t rWs = __builtin_amdgcn_make_buffer_rsrc((void*)wsp, 0, WS_USED, 0x00020000);
    u32 Ev = 0;
    auto after_barrier = [&](int s) {
      if (s == 0) {
        if (lw == 0) dma_word(0, rWs, WS_E + g * 128, lbase + SCR);
        asm volatile("buffer_load_dword %0, %1, %2, %3 offen sc1" : "+v"(Ev) : "v"(0u), "s"(rWs), "s"(WS_E + g * 128) : "memory");
        const v4u ones = {~0u, ~0u, ~0u, ~0u};
        asm volatile("buffer_store_dwordx4 %0, %1, %2, %3 offen sc1\n\ts_nop 1" ::"v"(ones), "v"(lane * 16 + 1024 * (lw & 1)), "s"(rWs),
                     "s"(WS_P + (bl * 32 + h) * 2048)
                     : "memory");
      }
      if (s == 1) {
        asm volatile("" : "+v"(Ev));
        const u32 ev = Ev + 1;
        if (lane == 0)
          asm volatile("buffer_store_dword %0, %1, %2, %3 offen sc1\n\ts_nop 1" ::"v"(ev), "v"(0u), "s"(rWs),
                       "s"(WS_FLAGP + ((bl * 32 + h) * 2 + (lw & 1)) * 128)
                       : "memory");
      }
      if (s == FDMA_S && lw == 0)
        dma_word((((lane >> 3) & 1) * 64 + (lane & 7)) * 128, rWs, WS_FLAGP + ((2 * g * 32 + 4 * j) * 2) * 128, lbase + KREG + 16384);
      if (s == PDMA_S) {
#pragma unroll
        for (int i = 0; i < 2; ++i)
#pragma unroll
          for (int qb = 0; qb < 2; ++qb) {
            const int hx = 2 * (lw & 1) + i;
            dma<true>(lane * 16, rWs, WS_P + (bl * 32 + 4 * j + ((hrot + hx) & 3)) * 2048 + 1024 * qb,
                      lbase + KREG + (((lw >> 1) * 4 + hx) * 2 + qb) * 1024);
          }
      }
      if (s < 8 && s == kpos) {
        const __amdgpu_buffer_rsrc_t rK = __builtin_amdgcn_make_buffer_rsrc((void*)kc, 0, 512 * 12288, 0x00020000);
#pragma unroll
        for (int c = 0; c < 3; ++c)
#pragma unroll
          for (int i = 0; i < 2; ++i) dma<false>(vKV[i], rK, soKV0 + h * 12288 + 128 * c, lbase + KREG + 8192 * c + lw * 2048 + 1024 * i);
      }
    };
    static_for<0, NST>([&](auto tc_) {
      constexpr int t = decltype(tc_)::value;
      constexpr u32 slot = (t % NSLOT) * SLOT;
      u32 soW, soA;
      if constexpr (t < 10) {
        soW = soW0 + kcur * (64 * 12288);
        soA = soA0 + kcur * 128;
        kcur = kcur == 9 ? 0 : kcur + 1;
      } else {
        if constexpr (t == 10) {
#pragma unroll
          for (int i = 0; i < 2; ++i) vA[i] = vKV[i];
#pragma unroll
          for (int i = 0; i < 6; ++i) vW[i] = kk[i] * 1280 + cc[i];
        }
        constexpr int hi = (t - 10) / 3, c0 = (t - 10) % 3;
        const int c = c0 + crot >= 3 ? c0 + crot - 3 : c0 + crot;
        const u32 hp = 4 * j + ((hrot + hi) & 3);
        soW = (hp * 192 + 64 * c + 16 * lw) * 1280 + 128 * L0;
        soA = soKV0 + hp * 12288 + 128 * c;
      }
      static_for<0, 8>([&](auto ic_) {
        constexpr int i = decltype(ic_)::value, p = 8 * t + i;
        if constexpr (t < 10) {
          if constexpr (i < 2) dma<false>(vA[i], rX, soA, slot + ldsA + 1024 * i);
          else dma<false>(vW[i - 2], rWq, soW, slot + ldsW + 1024 * (i - 2));
        } else {
          const __amdgpu_buffer_rsrc_t rV = __builtin_amdgcn_make_buffer_rsrc((void*)vc, 0, 512 * 12288, 0x00020000);
          const __amdgpu_buffer_rsrc_t rWo = __builtin_amdgcn_make_buffer_rsrc((void*)wo, 0, 6144 * 1280, 0x00020000);
          if constexpr (i < 2) dma<false>(vA[i], rV, soA, slot + ldsA + 1024 * i);
          else dma<false>(vW[i - 2], rWo, soW, slot + ldsW + 1024 * (i - 2));
        }
        if constexpr (p + 1 - LA >= 8 && (p + 1 - LA) % 8 == 0) {
          constexpr int s = (p + 1 - LA) / 8 - 1;
          wait_vm<LA>();
          bar();
          if constexpr (s < 8 || s == FDMA_S || s == PDMA_S) after_barrier(s);
        }
      });
    });
    static_for<(8 * NST - LA) / 8, NST>([&](auto sc_) {
      constexpr int s = decltype(sc_)::value;
      wait_vm<8 * (NST - 1 - s)>();
      bar();
    });
  };

  auto compute = [&]() __attribute__((always_inline)) {
    const int cw = wave, b = cw >> 1, half = cw & 1;
    const __amdgpu_buffer_rsrc_t rWs = __builtin_amdgcn_make_buffer_rsrc((void*)wsp, 0, WS_USED, 0x00020000);
    const int r16 = lane & 15, g4 = lane >> 4, q4 = (lane & 15) >> 2, p4 = lane & 3;
    const int x2 = (g4 & 1) | ((q4 >> 1) << 1);
    const u32 fsw = (r16 >> 1) & 7;
    u32 e = 0, Ev = 0;
    f32x4 accS[2][2];
    {
      f32x4 accQ[6][2];
      s16x4 kl[3][2], kh[3][2];
      u32 aW[6], aX[2], aY[2], aK[3][2];
#pragma unroll
      for (int tb = 0; tb < 6; ++tb) aW[tb] = lbase + RING + 384 * (8 * g4 + q4) + 32 * ((6 * half + tb) ^ x2) + 16 * (p4 >> 1) + 8 * (p4 & 1);
#pragma unroll
      for (int ks = 0; ks < 2; ++ks) {
        aX[ks] = lbase + RING + ACT + 128 * (32 * b + 16 * half + r16) + 16 * ((4 * ks + g4) ^ fsw);
        aY[ks] = lbase + RING + ACT + 128 * (32 * b + 16 * (half ^ 1) + r16) + 16 * ((4 * ks + g4) ^ fsw);
      }
#pragma unroll
      for (int u = 0; u < 3; ++u)
#pragma unroll
        for (int a = 0; a < 2; ++a) {
          const int ch = 12 * half + 4 * u + 2 * a + (g4 >> 1);
          aK[u][a] = lbase + KREG + 8192 * (ch >> 3) + 128 * (32 * b + r16) + 16 * ((ch & 7) ^ fsw) + 8 * (g4 & 1);
        }
#pragma unroll
      for (int i = 0; i < 6; ++i) asm volatile("" : "+v"(aW[i]));
#pragma unroll
      for (int i = 0; i < 2; ++i) asm volatile("" : "+v"(aX[i]), "+v"(aY[i]));
#pragma unroll
      for (int i = 0; i < 6; ++i) asm volatile("" : "+v"(aK[i >> 1][i & 1]));

      s16x4 wl[2][6], wh[2][6];
      bf16x8 xf[2][2];
      auto rd1 = [&](int n, int part) {
        const int set = n & 1, ks = n & 1;
        if (part == 0) xf[set][0] = lds128(aX[ks], 0);
        else if (part == 3) xf[set][1] = lds128(aY[ks], 0);
        else {
          const int q = part < 3 ? part - 1 : part - 2, tb = q >> 1;
          if (q & 1) wh[set][tb] = ldstr4(aW[tb], 384 * (32 * ks + 4));
          else wl[set][tb] = ldstr4(aW[tb], 384 * 32 * ks);
        }
      };
      auto rdk = [&](int m) {
        const int u = m >> 2, kb = (m >> 1) & 1, a = m & 1;
        const s16x4 v = *(LAS s16x4*)(lp(aK[u][a]) + 2048 * kb);
        if (a) kh[u][kb] = v; else kl[u][kb] = v;
      };
      auto adv1 = [&](int s) {
        const u32 d = (s % NSLOT == 0) ? (u32)(-(NSLOT - 1) * SLOT) : (u32)SLOT;
#pragma unroll
        for (int i = 0; i < 6; ++i) { aW[i] += d; asm volatile("" : "+v"(aW[i])); }
#pragma unroll
        for (int i = 0; i < 2; ++i) { aX[i] += d; aY[i] += d; asm volatile("" : "+v"(aX[i]), "+v"(aY[i])); }
      };
      auto mm1 = [&](int n, int tb, int qb) {
        const int set = n & 1;
        const f32x4 c0 = (n == 0) ? (f32x4){0.f, 0.f, 0.f, 0.f} : accQ[tb][qb];
        accQ[tb][qb] = mfma16(cat8(wl[set][tb], wh[set][tb]), xf[set][qb], c0);
      };
      bar();
#pragma unroll
      for (int q = 0; q < 14; ++q) rd1(0, q);
      SB();
#pragma unroll
      for (int n = 0; n < 19; ++n) {
        const int s = n >> 1, ks = n & 1;
        if (ks == 1) {
          wait_lgkm0();
          bar();
        }
#pragma unroll
        for (int m = 0; m < 12; ++m) {
          mm1(n, m >> 1, m & 1);
          SB();
#pragma unroll
          for (int q = 0; q < 14; ++q)
            if (q >= rsched1(ks, m) && q < rsched1(ks, m + 1)) rd1(n + 1, q);
          if (ks == 0 && s < 9 && m == 9) adv1(s + 1);
          SB();
        }
        if (n == EPO_N - 2) Ev = *(LAS u32*)(lp(lbase + SCR));
        if (n == EPO_N) {
          e = rfl(Ev) + 1;
          asm volatile("" : "+s"(e));
        }
        SB();
      }
      v4u qp[2];
      auto qpack = [&](int u, int qb) {
        qp[qb][0] = pk2(accQ[2 * u][qb][0], accQ[2 * u][qb][1]);
        qp[qb][1] = pk2(accQ[2 * u][qb][2], accQ[2 * u][qb][3]);
        qp[qb][2] = pk2(accQ[2 * u + 1][qb][0], accQ[2 * u + 1][qb][1]);
        qp[qb][3] = pk2(accQ[2 * u + 1][qb][2], accQ[2 * u + 1][qb][3]);
      };
      auto smm = [&](int u) {
#pragma unroll
        for (int qb = 1; qb >= 0; --qb)
#pragma unroll
          for (int kb = 0; kb < 2; ++kb) {
            const f32x4 c0 = u == 0 ? (f32x4){0.f, 0.f, 0.f, 0.f} : accS[kb][qb];
            accS[kb][qb] = mfma16(cat8(kl[u][kb], kh[u][kb]), __builtin_bit_cast(bf16x8, qp[qb]), c0);
          }
      };
#pragma unroll
      for (int m = 0; m < 12; ++m) {
        mm1(19, m >> 1, m & 1);
        SB();
        if (m < 6) { rdk(2 * m); rdk(2 * m + 1); }
        if (m == 5) qpack(0, 0);
        if (m == 6) qpack(0, 1);
        if (m == 7) smm(0);
        if (m == 9) qpack(1, 0);
        if (m == 10) qpack(1, 1);
        if (m == 11) smm(1);
        SB();
      }
      qpack(2, 0);
      qpack(2, 1);
      smm(2);
      const u32 scw = lbase + SCR + 4096 * b + 2048 * half + 16 * lane;
#pragma unroll
      for (int kb = 0; kb < 2; ++kb) *(LAS f32x4*)(lp(scw) + 1024 * kb) = accS[kb][1];
      wait_lgkm0();
    }
    const int c016 = (hh & 1) * 2;
    u32 aW[5], aV[2];
#pragma unroll
    for (int cb = 0; cb < 5; ++cb)
      aW[cb] = lbase + RING + SLOT * (10 % NSLOT) + 384 * (8 * g4 + q4) + 32 * ((c016 + 5 * half + cb) ^ x2) + 16 * (p4 >> 1) + 8 * (p4 & 1);
#pragma unroll
    for (int ks = 0; ks < 2; ++ks) aV[ks] = lbase + RING + SLOT * (10 % NSLOT) + ACT + 128 * (32 * b + r16) + 16 * ((4 * ks + g4) ^ fsw);
#pragma unroll
    for (int i = 0; i < 5; ++i) asm volatile("" : "+v"(aW[i]));
#pragma unroll
    for (int i = 0; i < 2; ++i) asm volatile("" : "+v"(aV[i]));
    bar();
    s16x4 wl[2][5], wh[2][5];
    bf16x8 vf[2][2];
    auto rd2 = [&](int n, int part) {
      const int set = n & 1, ks = n & 1;
      if (part == 0) vf[set][0] = lds128(aV[ks], 0);
      else if (part == 3) vf[set][1] = lds128(aV[ks], 2048);
      else {
        const int q = part < 3 ? part - 1 : part - 2, cb = q >> 1;
        if (q & 1) wh[set][cb] = ldstr4(aW[cb], 384 * (32 * ks + 4));
        else wl[set][cb] = ldstr4(aW[cb], 384 * 32 * ks);
      }
    };
    auto adv2 = [&](int s) {
      const u32 d = (s % NSLOT == 0) ? (u32)(-(NSLOT - 1) * SLOT) : (u32)SLOT;
#pragma unroll
      for (int i = 0; i < 5; ++i) { aW[i] += d; asm volatile("" : "+v"(aW[i])); }
#pragma unroll
      for (int i = 0; i < 2; ++i) { aV[i] += d; asm volatile("" : "+v"(aV[i])); }
    };
    f32x4 X[2][5];
    v4u Xp[4][5];
    v4u Pf[8];
    f32x4 O[5][2];
#pragma unroll
    for (int cb = 0; cb < 5; ++cb)
#pragma unroll
      for (int qb = 0; qb < 2; ++qb) O[cb][qb] = (f32x4){0.f, 0.f, 0.f, 0.f};
    auto mm2 = [&](int n, int cb, int rb) {
      const int set = n & 1;
      const f32x4 c0 = (n % 6 == 0) ? (f32x4){0.f, 0.f, 0.f, 0.f} : X[rb][cb];
      X[rb][cb] = mfma16(vf[set][rb], cat8(wl[set][cb], wh[set][cb]), c0);
    };
    auto pack = [&](int hi, int cb) {
      Xp[hi][cb][0] = pk2(X[0][cb][0], X[0][cb][1]);
      Xp[hi][cb][1] = pk2(X[0][cb][2], X[0][cb][3]);
      Xp[hi][cb][2] = pk2(X[1][cb][0], X[1][cb][1]);
      Xp[hi][cb][3] = pk2(X[1][cb][2], X[1][cb][3]);
      asm volatile("" : "+v"(Xp[hi][cb]));
    };
    auto pvi = [&](int k) {
      const int hi = k / 10, cb = (k % 10) >> 1, qb = k & 1;
      O[cb][qb] = mfma16(__builtin_bit_cast(bf16x8, Xp[hi][cb]), __builtin_bit_cast(bf16x8, Pf[2 * hi + qb]), O[cb][qb]);
    };
    float sv[8], mc = 0.f, sum = 0.f, t0 = 0.f, t1 = 0.f;
    v4u pfo;
    constexpr float scale = 0.07216878364870322f, cs = 1.4426950408889634f;
    {
      const u32 scr = lbase + SCR + 4096 * b + 2048 * (half ^ 1) + 16 * lane;
      const f32x4 o0 = *(LAS f32x4*)(lp(scr)), o1 = *(LAS f32x4*)(lp(scr) + 1024);
      SB();
#pragma unroll
      for (int q = 0; q < 12; ++q) rd2(0, q);
      SB();
#pragma unroll
      for (int r = 0; r < 4; ++r) {
        sv[r] = accS[0][0][r] + o0[r];
        sv[4 + r] = accS[1][0][r] + o1[r];
      }
#pragma unroll
      for (int d = 0; d < 4; ++d) {
        const u32 a = pk2(sv[2 * d], sv[2 * d + 1]);
        const u32 c = pk2(__uint_as_float(a << 16) * scale, __uint_as_float(a & 0xffff0000u) * scale);
        sv[2 * d] = __uint_as_float(c << 16);
        sv[2 * d + 1] = __uint_as_float(c & 0xffff0000u);
      }
      float m = fmaxf(fmaxf(fmaxf(sv[0], sv[1]), fmaxf(sv[2], sv[3])), fmaxf(fmaxf(sv[4], sv[5]), fmaxf(sv[6], sv[7])));
      const auto s1 = __builtin_amdgcn_permlane16_swap(__float_as_uint(m), __float_as_uint(m), false, false);
      m = fmaxf(__uint_as_float(s1[0]), __uint_as_float(s1[1]));
      const auto s2 = __builtin_amdgcn_permlane32_swap(__float_as_uint(m), __float_as_uint(m), false, false);
      m = fmaxf(__uint_as_float(s2[0]), __uint_as_float(s2[1]));
      mc = -m * cs;
    }
    SB();
    auto smx = [&](int n, int m) {
      if (n == 0) {
        if (m == 0 || m == 1) {
#pragma unroll
          for (int r = 0; r < 4; ++r) sv[4 * m + r] = __builtin_fmaf(sv[4 * m + r], cs, mc);
        } else if (m <= 5) {
          sv[2 * (m - 2)] = __builtin_amdgcn_exp2f(sv[2 * (m - 2)]);
          sv[2 * (m - 2) + 1] = __builtin_amdgcn_exp2f(sv[2 * (m - 2) + 1]);
        } else if (m == 6) {
          t0 = (sv[0] + sv[1]) + (sv[2] + sv[3]);
        } else if (m == 7) {
          t1 = (sv[4] + sv[5]) + (sv[6] + sv[7]);
          sum = t0 + t1;
        } else if (m == 8) {
          const auto s1 = __builtin_amdgcn_permlane16_swap(__float_as_uint(sum), __float_as_uint(sum), false, false);
          sum = __uint_as_float(s1[0]) + __uint_as_float(s1[1]);
        } else {
          const auto s2 = __builtin_amdgcn_permlane32_swap(__float_as_uint(sum), __float_as_uint(sum), false, false);
          sum = __builtin_amdgcn_rcpf(__uint_as_float(s2[0]) + __uint_as_float(s2[1]));
        }
      } else if (n == 1) {
        if (m == 1 || m == 2) {
#pragma unroll
          for (int r = 0; r < 4; ++r) sv[4 * (m - 1) + r] *= sum;
        } else if (m == 3) {
#pragma unroll
          for (int d = 0; d < 4; ++d) pfo[d] = pk2(sv[2 * d], sv[2 * d + 1]);
        } else if (m == 4) {
#pragma unroll
          for (int d = 0; d < 4; ++d) pfo[d] = pfo[d] < 0xfffffffeu ? pfo[d] : 0xfffffffeu;
        } else if (m == 5) {
          __builtin_amdgcn_raw_buffer_store_b128(pfo, rWs, lane * 16 + 1024 * half, WS_P + ((2 * g + b) * 32 + h) * 2048, 16  );
        }
      }
    };
    u32 fval = 0;
    static_for<0, 24>([&](auto nc_) {
      constexpr int n = decltype(nc_)::value;
      constexpr int s = 10 + (n >> 1), ks = n & 1, hi = n / 6;
      constexpr bool last = (n % 6 == 5);
      if (ks == 1 && s < NST - 1) {
        wait_lgkm0();
        bar();
      }
#pragma unroll
      for (int m = 0; m < 10; ++m) {
        mm2(n, m >> 1, m & 1);
        SB();
        if (n + 1 < 24) {
#pragma unroll
          for (int q = 0; q < 12; ++q)
            if (q >= rsched2(ks, m) && q < rsched2(ks, m + 1)) rd2(n + 1, q);
        }
        if (n < 2) smx(n, m);
        if (last) {
          if (m == 4) pack(hi, 0);
          if (m == 6) pack(hi, 1);
          if (m == 8) pack(hi, 2);
          if (m == 9) { pack(hi, 3); pack(hi, 4); }
        } else if (ks == 0 && s < NST - 1 && m == 8) adv2(s + 1);
        SB();
      }
      if (n == PCHK_N - 1) {
        fval = *(LAS u32*)(lp(lbase + KREG + 16384 + 4 * (8 * b + (lane & 7))));
#pragma unroll
        for (int i = 0; i < 8; ++i) Pf[i] = *(LAS v4u*)(lp(lbase + KREG + 16 * lane) + (b * 8 + i) * 1024);
      }
      if (n == PCHK_N) {
        asm volatile("" : "+v"(X[0][0]), "+v"(X[0][1]), "+v"(X[0][2]), "+v"(X[0][3]), "+v"(X[0][4]), "+v"(X[1][0]), "+v"(X[1][1]), "+v"(X[1][2]),
                     "+v"(X[1][3]), "+v"(X[1][4]));
        if (!(__all(fval == e) && all_written(Pf))) {
          poll_eq((GAS u32*)(ws + WS_FLAGP + (((2 * g + b) * 32 + 4 * j) * 2 + (lane & 7)) * 128), e);
          asm volatile("" ::: "memory");
          const u32 pb0 = WS_P + ((2 * g + b) * 32 + 4 * j) * 2048;
          u32 spins = 0;
          do {
            load4x2_sc1(Pf, lane * 16, rWs, pb0 + (hrot & 3) * 2048, pb0 + ((hrot + 1) & 3) * 2048, pb0 + ((hrot + 2) & 3) * 2048,
                        pb0 + ((hrot + 3) & 3) * 2048);
          } while (!all_written(Pf) && ++spins < SPIN_CAP);
        }
      }
#pragma unroll
      for (int k = 0; k < 40; ++k)
        if (k >= pvsched(n) && k < pvsched(n + 1)) pvi(k);
      SB();
    });
    {
      const u32 pos = (r16 >> 1) * 8 + (r16 & 1) * 4 + g4;
      const u32 sb = WS_SLAB + (w * 4 + cw) * 5120;
#pragma unroll
      for (int cb = 0; cb < 5; ++cb) {
        v4u v;
        v[0] = pk2(O[cb][0][0], O[cb][0][1]); v[1] = pk2(O[cb][0][2], O[cb][0][3]);
        v[2] = pk2(O[cb][1][0], O[cb][1][1]); v[3] = pk2(O[cb][1][2], O[cb][1][3]);
        __builtin_amdgcn_raw_buffer_store_b128(v, rWs, pos * 16 + 1024 * cb, sb, 16  );
      }
      wait_vm<0>();
      if (lane == 0) __hip_atomic_store((GAS u32*)(ws + WS_FLAGO + (w * 4 + cw) * 128), e, RLX_AGENT);
    }
    {
      const int w0 = w & ~7;
      const int cb = lane < 40 ? lane >> 3 : 4, rr = lane & 7;
      poll_eq((GAS u32*)(ws + WS_FLAGO + ((w0 + (lane & 7)) * 4 + cw) * 128), e);
      asm volatile("" ::: "memory");
      v4u v[8];
      load8_sc1(v, (8 * j + rr) * 16 + 1024 * cb, rWs, WS_SLAB + (w0 * 4 + cw) * 5120, 4 * 5120);
      if (lane < 40) {
        float a[8];
#pragma unroll
        for (int d = 0; d < 4; ++d) {
          float lo[8], hi[8];
#pragma unroll
          for (int i = 0; i < 8; ++i) {
            lo[i] = __uint_as_float(v[i][d] << 16);
            hi[i] = __uint_as_float(v[i][d] & 0xffff0000u);
          }
          a[2 * d] = ((lo[0] + lo[1]) + (lo[2] + lo[3])) + ((lo[4] + lo[5]) + (lo[6] + lo[7]));
          a[2 * d + 1] = ((hi[0] + hi[1]) + (hi[2] + hi[3])) + ((hi[4] + hi[5]) + (hi[6] + hi[7]));
        }
        const int qp = 2 * j + (rr >> 2), gg = rr & 3;
        const u32 col = 160 * hh + 80 * half + 16 * cb + 4 * gg;
        GAS char* o0 = (GAS char*)out + (size_t)((2 * g + b) * 32 + qp) * 1280 + col * 2;
        *(GAS v2u*)o0 = (v2u){pk2(a[0], a[1]), pk2(a[2], a[3])};
        *(GAS v2u*)(o0 + 16 * 1280) = (v2u){pk2(a[4], a[5]), pk2(a[6], a[7])};
      }
      if (j == 0 && hh == 0 && cw == 0 && lane == 0) __hip_atomic_store((GAS u32*)(ws + WS_E + g * 128), e, RLX_AGENT);
    }
    {
      const unsigned long long tend = __builtin_amdgcn_s_memrealtime();
      unsigned long long mn = ~0ull;
      for (int i = 0; i < 4; ++i) {
        unsigned long long v2 = __hip_atomic_load((GAS unsigned long long*)(ws + (20u << 20) + (i * 64 + lane) * 8), RLX_AGENT);
        mn = v2 < mn ? v2 : mn;
      }
      for (int o = 32; o >= 1; o >>= 1) { unsigned long long a = __shfl_xor(mn, o); mn = a < mn ? a : mn; }
      const unsigned long long d = (tend - mn) * 100;
      const unsigned long long t1 = __builtin_amdgcn_s_memrealtime();
      if (d < 2000000ull) while (__builtin_amdgcn_s_memrealtime() - t1 < d) __builtin_amdgcn_s_sleep(8);
    }
  };

  if (wave < 4) compute(); else loader();
}

extern "C" void kernel_launch(void* const* d_in, const int* in_sizes, int n_in, void* d_out, int out_size, void* d_ws, size_t ws_size,
                              hipStream_t stream) {
  (void)hipFuncSetAttribute((const void*)gqa_block_kernel, hipFuncAttributeMaxDynamicSharedMemorySize, LDS_BYTES);
  gqa_block_kernel<<<256, 512, LDS_BYTES, stream>>>((const char*)d_in[0], (const char*)d_in[1], (const char*)d_in[2], (const char*)d_in[3],
                                                     (const char*)d_in[4], (char*)d_out, (char*)d_ws);
}
```

```cpp
#include <hip/hip_runtime.h>
#include <hip/hip_bf16.h>
#include <stdint.h>

typedef unsigned u32;
typedef u32 v4u __attribute__((ext_vector_type(4)));
typedef u32 v2u __attribute__((ext_vector_type(2)));
typedef float f32x2 __attribute__((ext_vector_type(2)));
typedef float f32x4 __attribute__((ext_vector_type(4)));
typedef short s16x4 __attribute__((ext_vector_type(4)));
typedef __bf16 bf16x2 __attribute__((ext_vector_type(2)));
typedef __bf16 bf16x8 __attribute__((ext_vector_type(8)));
#define LAS __attribute__((address_space(3)))
#define GAS __attribute__((address_space(1)))
#define DI __device__ __forceinline__
#define RLX_AGENT __ATOMIC_RELAXED, __HIP_MEMORY_SCOPE_AGENT
#define SB() __builtin_amdgcn_sched_barrier(0)

namespace {
constexpr int SLOT = 32768, ACT = 24576, NSLOT = 3, KREG = 0, SCR = 24576, V0REG = 32768, RING = 65536, LDS_BYTES = RING + SLOT * NSLOT;
constexpr int NST = 22;
constexpr u32 WS_E = 0, WS_FLAGP = 4096, WS_FLAGO = 135168, WS_P = 266240, WS_SLAB = 1314816;
constexpr u32 WS_USED = WS_SLAB + 256 * 20480;
constexpr u32 SPIN_CAP = 1u << 18;
constexpr int np(int t) { return t >= 10 && t <= 12 ? 6 : 8; }
constexpr int EPO_N = 4;
constexpr int FDMA_S = 10;
constexpr int PDMA_S = 15;
constexpr int PCHK_N = 14;

template <bool SC1>
DI void dma(u32 voff, __amdgpu_buffer_rsrc_t rs, u32 soff, u32 ldsdst) {
  if constexpr (SC1)
    asm volatile("s_mov_b32 m0, %2\n\ts_nop 0\n\tbuffer_load_dwordx4 %0, %1, %3 offen sc1 lds" ::"v"(voff), "s"(rs), "s"(ldsdst), "s"(soff) : "memory");
  else
    asm volatile("s_mov_b32 m0, %2\n\ts_nop 0\n\tbuffer_load_dwordx4 %0, %1, %3 offen lds" ::"v"(voff), "s"(rs), "s"(ldsdst), "s"(soff) : "memory");
}
DI void dma_word(u32 voff, __amdgpu_buffer_rsrc_t rs, u32 soff, u32 ldsdst) {
  asm volatile("s_mov_b32 m0, %2\n\ts_nop 0\n\tbuffer_load_dword %0, %1, %3 offen sc1 lds" ::"v"(voff), "s"(rs), "s"(ldsdst), "s"(soff) : "memory");
}
template <int N> DI void wait_vm() { asm volatile("s_waitcnt vmcnt(%0)" ::"i"(N) : "memory"); }
DI void bar() {
  SB();
  asm volatile("s_barrier" ::: "memory");
  SB();
}
DI void wait_lgkm0() {
  asm volatile("" ::: "memory");
  __builtin_amdgcn_s_waitcnt(0xC07F);
  asm volatile("" ::: "memory");
}
DI u32 rfl(u32 v) { return __builtin_amdgcn_readfirstlane(v); }
DI u32 pk2(float a, float b) {
  f32x2 v = {a, b};
  return __builtin_bit_cast(u32, __builtin_convertvector(v, bf16x2));
}
DI LAS char* lp(u32 a) { return (LAS char*)a; }
DI bf16x8 lds128(u32 a, int off) { return *(LAS bf16x8*)(lp(a) + off); }
DI s16x4 ldstr4(u32 a, int off) { return __builtin_amdgcn_ds_read_tr16_b64_v4i16((LAS s16x4*)(lp(a) + off)); }
DI bf16x8 cat8(s16x4 a, s16x4 b) { return __builtin_bit_cast(bf16x8, __builtin_shufflevector(a, b, 0, 1, 2, 3, 4, 5, 6, 7)); }
DI f32x4 mfma16(bf16x8 a, bf16x8 b, f32x4 c) { return __builtin_amdgcn_mfma_f32_16x16x32_bf16(a, b, c, 0, 0, 0); }

template <int V> struct IC { static constexpr int value = V; };
template <int I, int N, class F> DI void static_for(F&& f) {
  if constexpr (I < N) {
    f(IC<I>{});
    static_for<I + 1, N>(f);
  }
}
constexpr int rsched1(int ks, int m) { return ks == 0 ? (m <= 7 ? 2 * m : 14) : (m == 0 ? 0 : (m <= 4 ? m + 1 : (m <= 12 ? m + 2 : 14))); }
constexpr int rsched2(int ks, int m) { return ks == 0 ? (m <= 4 ? 2 * m : (m <= 8 ? m + 4 : 12)) : (m == 0 ? 0 : (m <= 4 ? m + 1 : m + 2)); }
constexpr int pvsched(int n) {
  if (n <= PCHK_N) return 0;
  if (n <= 18) { const int a = (20 * (n - PCHK_N - 1) + (17 - PCHK_N)) / (17 - PCHK_N); return a > 20 ? 20 : a; }
  if (n <= 23) return 20 + 2 * (n - 18);
  return 40;
}
static_assert(pvsched(18) == 20 && pvsched(23) == 30 && pvsched(24) == 40 && PCHK_N >= 11 && PCHK_N <= 16, "");
static_assert(2 * (PDMA_S + 1 - 10) - 1 <= PCHK_N - 1 && FDMA_S < PDMA_S && FDMA_S >= 10, "flags before fragments, both landed before they are read");

DI void load8_sc1(v4u (&v)[8], u32 vo, __amdgpu_buffer_rsrc_t rs, u32 s0, u32 step) {
  u32 s1 = s0 + step, s2 = s1 + step, s3 = s2 + step, s4 = s3 + step, s5 = s4 + step, s6 = s5 + step, s7 = s6 + step;
  asm volatile(
      "s_nop 4\n\t"
      "buffer_load_dwordx4 %0, %8, %9, %10 offen sc1\n\t"
      "buffer_load_dwordx4 %1, %8, %9, %11 offen sc1\n\t"
      "buffer_load_dwordx4 %2, %8, %9, %12 offen sc1\n\t"
      "buffer_load_dwordx4 %3, %8, %9, %13 offen sc1\n\t"
      "buffer_load_dwordx4 %4, %8, %9, %14 offen sc1\n\t"
      "buffer_load_dwordx4 %5, %8, %9, %15 offen sc1\n\t"
      "buffer_load_dwordx4 %6, %8, %9, %16 offen sc1\n\t"
      "buffer_load_dwordx4 %7, %8, %9, %17 offen sc1\n\t"
      "s_waitcnt vmcnt(0)"
      : "=&v"(v[0]), "=&v"(v[1]), "=&v"(v[2]), "=&v"(v[3]), "=&v"(v[4]), "=&v"(v[5]), "=&v"(v[6]), "=&v"(v[7])
      : "v"(vo), "s"(rs), "s"(s0), "s"(s1), "s"(s2), "s"(s3), "s"(s4), "s"(s5), "s"(s6), "s"(s7)
      : "memory");
}
DI void load4x2_sc1(v4u (&v)[8], u32 vo, __amdgpu_buffer_rsrc_t rs, u32 s0, u32 s1, u32 s2, u32 s3) {
  asm volatile(
      "s_nop 4\n\t"
      "buffer_load_dwordx4 %0, %8, %9, %10 offen sc1\n\t"
      "buffer_load_dwordx4 %1, %8, %9, %10 offen offset:1024 sc1\n\t"
      "buffer_load_dwordx4 %2, %8, %9, %11 offen sc1\n\t"
      "buffer_load_dwordx4 %3, %8, %9, %11 offen offset:1024 sc1\n\t"
      "buffer_load_dwordx4 %4, %8, %9, %12 offen sc1\n\t"
      "buffer_load_dwordx4 %5, %8, %9, %12 offen offset:1024 sc1\n\t"
      "buffer_load_dwordx4 %6, %8, %9, %13 offen sc1\n\t"
      "buffer_load_dwordx4 %7, %8, %9, %13 offen offset:1024 sc1\n\t"
      "s_waitcnt vmcnt(0)"
      : "=&v"(v[0]), "=&v"(v[1]), "=&v"(v[2]), "=&v"(v[3]), "=&v"(v[4]), "=&v"(v[5]), "=&v"(v[6]), "=&v"(v[7])
      : "v"(vo), "s"(rs), "s"(s0), "s"(s1), "s"(s2), "s"(s3)
      : "memory");
}
DI void poll_eq(GAS u32* p, u32 want) {
  for (u32 spins = 0; spins < SPIN_CAP; ++spins) {
    const u32 v = __hip_atomic_load(p, RLX_AGENT);
    if (__all(v == want)) break;
    __builtin_amdgcn_s_sleep(1);
  }
}
DI bool all_written(const v4u (&v)[8]) {
  u32 mx = 0;
#pragma unroll
  for (int i = 0; i < 8; ++i) {
    const u32 a = v[i][0] > v[i][1] ? v[i][0] : v[i][1], c = v[i][2] > v[i][3] ? v[i][2] : v[i][3];
    const u32 d = a > c ? a : c;
    mx = mx > d ? mx : d;
  }
  return __all(mx != 0xffffffffu);
}
}

__global__ __launch_bounds__(512, 2) void gqa_block_kernel(const char* __restrict__ x, const char* __restrict__ kc, const char* __restrict__ vc,
                                                           const char* __restrict__ wq, const char* __restrict__ wo, char* __restrict__ out,
                                                           char* wsp) {
  extern __shared__ __attribute__((aligned(16))) char smem_[];
  const int w = blockIdx.x;
  const int wave = rfl(threadIdx.x >> 6);
  const int lane = threadIdx.x & 63;
  const int j = w & 7, g = w >> 5, hh = (w >> 3) & 3;
  const int h = 4 * j + hh;
  const int hrot = hh + g;
  GAS char* ws = (GAS char*)wsp;
  const u32 lbase = (u32)(size_t)(LAS char*)smem_;

  {
    const unsigned long long tstart = __builtin_amdgcn_s_memrealtime();
    if (threadIdx.x == 0) __hip_atomic_store((GAS unsigned long long*)(ws + (20u << 20) + w * 8), tstart, RLX_AGENT);
  }
  auto loader = [&]() __attribute__((always_inline)) {
    const int lw = wave - 4;
    __builtin_amdgcn_s_setprio(1);
    const __amdgpu_buffer_rsrc_t rX = __builtin_amdgcn_make_buffer_rsrc((void*)x, 0, 512 * 1280, 0x00020000);
    const __amdgpu_buffer_rsrc_t rWq = __builtin_amdgcn_make_buffer_rsrc((void*)wq, 0, 640 * 12288, 0x00020000);
    u32 ra[2], ca[2], vA[2], vKV[2];
#pragma unroll
    for (int i = 0; i < 2; ++i) {
      ra[i] = 8 * i + (lane >> 3);
      ca[i] = 16 * ((lane & 7) ^ ((ra[i] >> 1) & 7));
      vA[i] = ra[i] * 1280 + ca[i];
    }
    u32 kk[6], cc[6], vW[6];
#pragma unroll
    for (int i = 0; i < 6; ++i) {
      const u32 L = 8 * i + (lane >> 3), k = (L * 43) >> 7;
      kk[i] = k;
      cc[i] = 128 * (L - 3 * k) + 16 * ((lane & 7) ^ ((k & 2) << 1) ^ ((k & 8) >> 2));
      vW[i] = k * 12288 + cc[i];
    }
#pragma unroll
    for (int i = 0; i < 2; ++i) vKV[i] = ra[i] * 384 + ca[i];
    const u32 ldsW = lbase + RING + lw * 6144, ldsA = lbase + RING + ACT + lw * 2048;
    const u32 soW0 = h * 384 + 16 * lw * 12288, soA0 = (64 * g + 16 * lw) * 1280;
    const int bl = 2 * g + (lw >> 1);
    const u32 soKV0 = (u32)(bl * 32 * 32 + 16 * (lw & 1)) * 384;
    const u32 L0 = (5 * hh) >> 1;
    int kcur = (g + 3 * hh) % 10;
    const int crot = g >> 2;
    const int kpos = ((w >> 3) + j) & 7, vpos = (kpos + 2) & 7;
    const __amdgpu_buffer_rsrc_t rWs = __builtin_amdgcn_make_buffer_rsrc((void*)wsp, 0, WS_USED, 0x00020000);
    u32 Ev = 0;
    auto after_barrier = [&](int s) {
      if (s == 0) {
        if (lw == 0) dma_word(0, rWs, WS_E + g * 128, lbase + SCR);
        asm volatile("buffer_load_dword %0, %1, %2, %3 offen sc1" : "+v"(Ev) : "v"(0u), "s"(rWs), "s"(WS_E + g * 128) : "memory");
        const v4u ones = {~0u, ~0u, ~0u, ~0u};
        asm volatile("buffer_store_dwordx4 %0, %1, %2, %3 offen sc1\n\ts_nop 1" ::"v"(ones), "v"(lane * 16 + 1024 * (lw & 1)), "s"(rWs),
                     "s"(WS_P + (bl * 32 + h) * 2048)
                     : "memory");
      }
      if (s == 1) {
        asm volatile("" : "+v"(Ev));
        const u32 ev = Ev + 1;
        if (lane == 0)
          asm volatile("buffer_store_dword %0, %1, %2, %3 offen sc1\n\ts_nop 1" ::"v"(ev), "v"(0u), "s"(rWs),
                       "s"(WS_FLAGP + ((bl * 32 + h) * 2 + (lw & 1)) * 128)
                       : "memory");
      }
      if (s == FDMA_S && lw == 0)
        dma_word((((lane >> 3) & 1) * 64 + (lane & 7)) * 128, rWs, WS_FLAGP + ((2 * g * 32 + 4 * j) * 2) * 128, lbase + KREG + 16384);
      if (s == PDMA_S) {
#pragma unroll
        for (int i = 0; i < 2; ++i)
#pragma unroll
          for (int qb = 0; qb < 2; ++qb) {
            const int hx = 2 * (lw & 1) + i;
            dma<true>(lane * 16, rWs, WS_P + (bl * 32 + 4 * j + ((hrot + hx) & 3)) * 2048 + 1024 * qb,
                      lbase + KREG + (((lw >> 1) * 4 + hx) * 2 + qb) * 1024);
          }
      }
      if (s < 8 && s == kpos) {
        const __amdgpu_buffer_rsrc_t rK = __builtin_amdgcn_make_buffer_rsrc((void*)kc, 0, 512 * 12288, 0x00020000);
#pragma unroll
        for (int c = 0; c < 3; ++c)
#pragma unroll
          for (int i = 0; i < 2; ++i) dma<false>(vKV[i], rK, soKV0 + h * 12288 + 128 * c, lbase + KREG + 8192 * c + lw * 2048 + 1024 * i);
      }
      if (s < 8 && s == vpos) {
        const __amdgpu_buffer_rsrc_t rV = __builtin_amdgcn_make_buffer_rsrc((void*)vc, 0, 512 * 12288, 0x00020000);
#pragma unroll
        for (int c0 = 0; c0 < 3; ++c0) {
          const int c = c0 + crot >= 3 ? c0 + crot - 3 : c0 + crot;
#pragma unroll
          for (int i = 0; i < 2; ++i)
            dma<false>(vKV[i], rV, soKV0 + (4 * j + (hrot & 3)) * 12288 + 128 * c, lbase + V0REG + 8192 * c0 + lw * 2048 + 1024 * i);
        }
      }
    };
    static_for<0, NST>([&](auto tc_) {
      constexpr int t = decltype(tc_)::value;
      constexpr u32 slot = (t % NSLOT) * SLOT;
      u32 soW, soA;
      if constexpr (t < 10) {
        soW = soW0 + kcur * (64 * 12288);
        soA = soA0 + kcur * 128;
        kcur = kcur == 9 ? 0 : kcur + 1;
      } else {
        if constexpr (t == 10) {
#pragma unroll
          for (int i = 0; i < 2; ++i) vA[i] = vKV[i];
#pragma unroll
          for (int i = 0; i < 6; ++i) vW[i] = kk[i] * 1280 + cc[i];
        }
        constexpr int hi = (t - 10) / 3, c0 = (t - 10) % 3;
        const int c = c0 + crot >= 3 ? c0 + crot - 3 : c0 + crot;
        const u32 hp = 4 * j + ((hrot + hi) & 3);
        soW = (hp * 192 + 64 * c + 16 * lw) * 1280 + 128 * L0;
        soA = soKV0 + hp * 12288 + 128 * c;
      }
      static_for<8 - np(t), 8>([&](auto ic_) {
        constexpr int i = decltype(ic_)::value;
        if constexpr (t < 10) {
          if constexpr (i < 2) dma<false>(vA[i], rX, soA, slot + ldsA + 1024 * i);
          else dma<false>(vW[i - 2], rWq, soW, slot + ldsW + 1024 * (i - 2));
        } else {
          const __amdgpu_buffer_rsrc_t rV = __builtin_amdgcn_make_buffer_rsrc((void*)vc, 0, 512 * 12288, 0x00020000);
          const __amdgpu_buffer_rsrc_t rWo = __builtin_amdgcn_make_buffer_rsrc((void*)wo, 0, 6144 * 1280, 0x00020000);
          if constexpr (i < 2) dma<false>(vA[i], rV, soA, slot + ldsA + 1024 * i);
          else dma<false>(vW[i - 2], rWo, soW, slot + ldsW + 1024 * (i - 2));
        }
      });
      if constexpr (t >= 1) {
        constexpr int s = t - 1;
        wait_vm<np(t)>();
        bar();
        if constexpr (s < 8 || s == FDMA_S || s == PDMA_S) after_barrier(s);
      }
    });
    wait_vm<0>();
    bar();
  };

  auto compute = [&]() __attribute__((always_inline)) {
    const int cw = wave, b = cw >> 1, half = cw & 1;
    const __amdgpu_buffer_rsrc_t rWs = __builtin_amdgcn_make_buffer_rsrc((void*)wsp, 0, WS_USED, 0x00020000);
    const int r16 = lane & 15, g4 = lane >> 4, q4 = (lane & 15) >> 2, p4 = lane & 3;
    const int x2 = (g4 & 1) | ((q4 >> 1) << 1);
    const u32 fsw = (r16 >> 1) & 7;
    u32 e = 0, Ev = 0;
    f32x4 accS[2][2];
    {
      f32x4 accQ[6][2];
      s16x4 kl[3][2], kh[3][2];
      u32 aW[6], aX[2], aY[2], aK[3][2];
#pragma unroll
      for (int tb = 0; tb < 6; ++tb) aW[tb] = lbase + RING + 384 * (8 * g4 + q4) + 32 * ((6 * half + tb) ^ x2) + 16 * (p4 >> 1) + 8 * (p4 & 1);
#pragma unroll
      for (int ks = 0; ks < 2; ++ks) {
        aX[ks] = lbase + RING + ACT + 128 * (32 * b + 16 * half + r16) + 16 * ((4 * ks + g4) ^ fsw);
        aY[ks] = lbase + RING + ACT + 128 * (32 * b + 16 * (half ^ 1) + r16) + 16 * ((4 * ks + g4) ^ fsw);
      }
#pragma unroll
      for (int u = 0; u < 3; ++u)
#pragma unroll
        for (int a = 0; a < 2; ++a) {
          const int ch = 12 * half + 4 * u + 2 * a + (g4 >> 1);
          aK[u][a] = lbase + KREG + 8192 * (ch >> 3) + 128 * (32 * b + r16) + 16 * ((ch & 7) ^ fsw) + 8 * (g4 & 1);
        }
#pragma unroll
      for (int i = 0; i < 6; ++i) asm volatile("" : "+v"(aW[i]));
#pragma unroll
      for (int i = 0; i < 2; ++i) asm volatile("" : "+v"(aX[i]), "+v"(aY[i]));
#pragma unroll
      for (int i = 0; i < 6; ++i) asm volatile("" : "+v"(aK[i >> 1][i & 1]));

      s16x4 wl[2][6], wh[2][6];
      bf16x8 xf[2][2];
      auto rd1 = [&](int n, int part) {
        const int set = n & 1, ks = n & 1;
        if (part == 0) xf[set][0] = lds128(aX[ks], 0);
        else if (part == 3) xf[set][1] = lds128(aY[ks], 0);
        else {
          const int q = part < 3 ? part - 1 : part - 2, tb = q >> 1;
          if (q & 1) wh[set][tb] = ldstr4(aW[tb], 384 * (32 * ks + 4));
          else wl[set][tb] = ldstr4(aW[tb], 384 * 32 * ks);
        }
      };
      auto rdk = [&](int m) {
        const int u = m >> 2, kb = (m >> 1) & 1, a = m & 1;
        const s16x4 v = *(LAS s16x4*)(lp(aK[u][a]) + 2048 * kb);
        if (a) kh[u][kb] = v; else kl[u][kb] = v;
      };
      auto adv1 = [&](int s) {
        const u32 d = (s % NSLOT == 0) ? (u32)(-(NSLOT - 1) * SLOT) : (u32)SLOT;
#pragma unroll
        for (int i = 0; i < 6; ++i) { aW[i] += d; asm volatile("" : "+v"(aW[i])); }
#pragma unroll
        for (int i = 0; i < 2; ++i) { aX[i] += d; aY[i] += d; asm volatile("" : "+v"(aX[i]), "+v"(aY[i])); }
      };
      auto mm1 = [&](int n, int tb, int qb) {
        const int set = n & 1;
        const f32x4 c0 = (n == 0) ? (f32x4){0.f, 0.f, 0.f, 0.f} : accQ[tb][qb];
        accQ[tb][qb] = mfma16(cat8(wl[set][tb], wh[set][tb]), xf[set][qb], c0);
      };
      bar();
#pragma unroll
      for (int q = 0; q < 14; ++q) rd1(0, q);
      SB();
#pragma unroll
      for (int n = 0; n < 19; ++n) {
        const int s = n >> 1, ks = n & 1;
        if (ks == 1) {
          wait_lgkm0();
          bar();
        }
#pragma unroll
        for (int m = 0; m < 12; ++m) {
          mm1(n, m >> 1, m & 1);
          SB();
#pragma unroll
          for (int q = 0; q < 14; ++q)
            if (q >= rsched1(ks, m) && q < rsched1(ks, m + 1)) rd1(n + 1, q);
          if (ks == 0 && s < 9 && m == 9) adv1(s + 1);
          SB();
        }
        if (n == EPO_N - 2) Ev = *(LAS u32*)(lp(lbase + SCR));
        if (n == EPO_N) {
          e = rfl(Ev) + 1;
          asm volatile("" : "+s"(e));
        }
        SB();
      }
      v4u qp[2];
      auto qpack = [&](int u, int qb) {
        qp[qb][0] = pk2(accQ[2 * u][qb][0], accQ[2 * u][qb][1]);
        qp[qb][1] = pk2(accQ[2 * u][qb][2], accQ[2 * u][qb][3]);
        qp[qb][2] = pk2(accQ[2 * u + 1][qb][0], accQ[2 * u + 1][qb][1]);
        qp[qb][3] = pk2(accQ[2 * u + 1][qb][2], accQ[2 * u + 1][qb][3]);
      };
      auto smm = [&](int u) {
#pragma unroll
        for (int qb = 1; qb >= 0; --qb)
#pragma unroll
          for (int kb = 0; kb < 2; ++kb) {
            const f32x4 c0 = u == 0 ? (f32x4){0.f, 0.f, 0.f, 0.f} : accS[kb][qb];
            accS[kb][qb] = mfma16(cat8(kl[u][kb], kh[u][kb]), __builtin_bit_cast(bf16x8, qp[qb]), c0);
          }
      };
#pragma unroll
      for (int m = 0; m < 12; ++m) {
        mm1(19, m >> 1, m & 1);
        SB();
        if (m < 6) { rdk(2 * m); rdk(2 * m + 1); }
        if (m == 5) qpack(0, 0);
        if (m == 6) qpack(0, 1);
        if (m == 7) smm(0);
        if (m == 9) qpack(1, 0);
        if (m == 10) qpack(1, 1);
        if (m == 11) smm(1);
        SB();
      }
      qpack(2, 0);
      qpack(2, 1);
      smm(2);
      const u32 scw = lbase + SCR + 4096 * b + 2048 * half + 16 * lane;
#pragma unroll
      for (int kb = 0; kb < 2; ++kb) *(LAS f32x4*)(lp(scw) + 1024 * kb) = accS[kb][1];
      wait_lgkm0();
    }
    const int c016 = (hh & 1) * 2;
    u32 aW[5], aV[2];
#pragma unroll
    for (int cb = 0; cb < 5; ++cb)
      aW[cb] = lbase + RING + SLOT * (10 % NSLOT) + 384 * (8 * g4 + q4) + 32 * ((c016 + 5 * half + cb) ^ x2) + 16 * (p4 >> 1) + 8 * (p4 & 1);
#pragma unroll
    for (int ks = 0; ks < 2; ++ks) aV[ks] = lbase + V0REG + 128 * (32 * b + r16) + 16 * ((4 * ks + g4) ^ fsw);
#pragma unroll
    for (int i = 0; i < 5; ++i) asm volatile("" : "+v"(aW[i]));
#pragma unroll
    for (int i = 0; i < 2; ++i) asm volatile("" : "+v"(aV[i]));
    bar();
    s16x4 wl[2][5], wh[2][5];
    bf16x8 vf[2][2];
    auto rd2 = [&](int n, int part) {
      const int set = n & 1, ks = n & 1;
      if (part == 0) vf[set][0] = lds128(aV[ks], 0);
      else if (part == 3) vf[set][1] = lds128(aV[ks], 2048);
      else {
        const int q = part < 3 ? part - 1 : part - 2, cb = q >> 1;
        if (q & 1) wh[set][cb] = ldstr4(aW[cb], 384 * (32 * ks + 4));
        else wl[set][cb] = ldstr4(aW[cb], 384 * 32 * ks);
      }
    };
    auto adv2 = [&](int s) {
      const u32 d = (s % NSLOT == 0) ? (u32)(-(NSLOT - 1) * SLOT) : (u32)SLOT;
      const u32 dv = s <= 12 ? 8192u : (s == 13 ? (u32)(RING + SLOT * (13 % NSLOT) + ACT - (V0REG + 2 * 8192)) : d);
#pragma unroll
      for (int i = 0; i < 5; ++i) { aW[i] += d; asm volatile("" : "+v"(aW[i])); }
#pragma unroll
      for (int i = 0; i < 2; ++i) { aV[i] += dv; asm volatile("" : "+v"(aV[i])); }
    };
    f32x4 X[2][5];
    v4u Xp[4][5];
    v4u Pf[8];
    f32x4 O[5][2];
#pragma unroll
    for (int cb = 0; cb < 5; ++cb)
#pragma unroll
      for (int qb = 0; qb < 2; ++qb) O[cb][qb] = (f32x4){0.f, 0.f, 0.f, 0.f};
    auto mm2 = [&](int n, int cb, int rb) {
      const int set = n & 1;
      const f32x4 c0 = (n % 6 == 0) ? (f32x4){0.f, 0.f, 0.f, 0.f} : X[rb][cb];
      X[rb][cb] = mfma16(vf[set][rb], cat8(wl[set][cb], wh[set][cb]), c0);
    };
    auto pack = [&](int hi, int cb) {
      Xp[hi][cb][0] = pk2(X[0][cb][0], X[0][cb][1]);
      Xp[hi][cb][1] = pk2(X[0][cb][2], X[0][cb][3]);
      Xp[hi][cb][2] = pk2(X[1][cb][0], X[1][cb][1]);
      Xp[hi][cb][3] = pk2(X[1][cb][2], X[1][cb][3]);
      asm volatile("" : "+v"(Xp[hi][cb]));
    };
    auto pvi = [&](int k) {
      const int hi = k / 10, cb = (k % 10) >> 1, qb = k & 1;
      O[cb][qb] = mfma16(__builtin_bit_cast(bf16x8, Xp[hi][cb]), __builtin_bit_cast(bf16x8, Pf[2 * hi + qb]), O[cb][qb]);
    };
    float sv[8], mc = 0.f, sum = 0.f, t0 = 0.f, t1 = 0.f;
    v4u pfo;
    constexpr float scale = 0.07216878364870322f, cs = 1.4426950408889634f;
    {
      const u32 scr = lbase + SCR + 4096 * b + 2048 * (half ^ 1) + 16 * lane;
      const f32x4 o0 = *(LAS f32x4*)(lp(scr)), o1 = *(LAS f32x4*)(lp(scr) + 1024);
      SB();
#pragma unroll
      for (int q = 0; q < 12; ++q) rd2(0, q);
      SB();
#pragma unroll
      for (int r = 0; r < 4; ++r) {
        sv[r] = accS[0][0][r] + o0[r];
        sv[4 + r] = accS[1][0][r] + o1[r];
      }
#pragma unroll
      for (int d = 0; d < 4; ++d) {
        const u32 a = pk2(sv[2 * d], sv[2 * d + 1]);
        const u32 c = pk2(__uint_as_float(a << 16) * scale, __uint_as_float(a & 0xffff0000u) * scale);
        sv[2 * d] = __uint_as_float(c << 16);
        sv[2 * d + 1] = __uint_as_float(c & 0xffff0000u);
      }
      float m = fmaxf(fmaxf(fmaxf(sv[0], sv[1]), fmaxf(sv[2], sv[3])), fmaxf(fmaxf(sv[4], sv[5]), fmaxf(sv[6], sv[7])));
      const auto s1 = __builtin_amdgcn_permlane16_swap(__float_as_uint(m), __float_as_uint(m), false, false);
      m = fmaxf(__uint_as_float(s1[0]), __uint_as_float(s1[1]));
      const auto s2 = __builtin_amdgcn_permlane32_swap(__float_as_uint(m), __float_as_uint(m), false, false);
      m = fmaxf(__uint_as_float(s2[0]), __uint_as_float(s2[1]));
      mc = -m * cs;
    }
    SB();
    auto smx = [&](int n, int m) {
      if (n == 0) {
        if (m == 0 || m == 1) {
#pragma unroll
          for (int r = 0; r < 4; ++r) sv[4 * m + r] = __builtin_fmaf(sv[4 * m + r], cs, mc);
        } else if (m <= 5) {
          sv[2 * (m - 2)] = __builtin_amdgcn_exp2f(sv[2 * (m - 2)]);
          sv[2 * (m - 2) + 1] = __builtin_amdgcn_exp2f(sv[2 * (m - 2) + 1]);
        } else if (m == 6) {
          t0 = (sv[0] + sv[1]) + (sv[2] + sv[3]);
        } else if (m == 7) {
          t1 = (sv[4] + sv[5]) + (sv[6] + sv[7]);
          sum = t0 + t1;
        } else if (m == 8) {
          const auto s1 = __builtin_amdgcn_permlane16_swap(__float_as_uint(sum), __float_as_uint(sum), false, false);
          sum = __uint_as_float(s1[0]) + __uint_as_float(s1[1]);
        } else {
          const auto s2 = __builtin_amdgcn_permlane32_swap(__float_as_uint(sum), __float_as_uint(sum), false, false);
          sum = __builtin_amdgcn_rcpf(__uint_as_float(s2[0]) + __uint_as_float(s2[1]));
        }
      } else if (n == 1) {
        if (m == 1 || m == 2) {
#pragma unroll
          for (int r = 0; r < 4; ++r) sv[4 * (m - 1) + r] *= sum;
        } else if (m == 3) {
#pragma unroll
          for (int d = 0; d < 4; ++d) pfo[d] = pk2(sv[2 * d], sv[2 * d + 1]);
        } else if (m == 4) {
#pragma unroll
          for (int d = 0; d < 4; ++d) pfo[d] = pfo[d] < 0xfffffffeu ? pfo[d] : 0xfffffffeu;
        } else if (m == 5) {
          __builtin_amdgcn_raw_buffer_store_b128(pfo, rWs, lane * 16 + 1024 * half, WS_P + ((2 * g + b) * 32 + h) * 2048, 16  );
        }
      }
    };
    u32 fval = 0;
    static_for<0, 24>([&](auto nc_) {
      constexpr int n = decltype(nc_)::value;
      constexpr int s = 10 + (n >> 1), ks = n & 1, hi = n / 6;
      constexpr bool last = (n % 6 == 5);
      if (ks == 1 && s < NST - 1) {
        wait_lgkm0();
        bar();
      }
#pragma unroll
      for (int m = 0; m < 10; ++m) {
        mm2(n, m >> 1, m & 1);
        SB();
        if (n + 1 < 24) {
#pragma unroll
          for (int q = 0; q < 12; ++q)
            if (q >= rsched2(ks, m) && q < rsched2(ks, m + 1)) rd2(n + 1, q);
        }
        if (n < 2) smx(n, m);
        if (last) {
          if (m == 4) pack(hi, 0);
          if (m == 6) pack(hi, 1);
          if (m == 8) pack(hi, 2);
          if (m == 9) { pack(hi, 3); pack(hi, 4); }
        } else if (ks == 0 && s < NST - 1 && m == 8) adv2(s + 1);
        SB();
      }
      if (n == PCHK_N - 1) {
        fval = *(LAS u32*)(lp(lbase + KREG + 16384 + 4 * (8 * b + (lane & 7))));
#pragma unroll
        for (int i = 0; i < 8; ++i) Pf[i] = *(LAS v4u*)(lp(lbase + KREG + 16 * lane) + (b * 8 + i) * 1024);
      }
      if (n == PCHK_N) {
        asm volatile("" : "+v"(X[0][0]), "+v"(X[0][1]), "+v"(X[0][2]), "+v"(X[0][3]), "+v"(X[0][4]), "+v"(X[1][0]), "+v"(X[1][1]), "+v"(X[1][2]),
                     "+v"(X[1][3]), "+v"(X[1][4]));
        if (!(__all(fval == e) && all_written(Pf))) {
          poll_eq((GAS u32*)(ws + WS_FLAGP + (((2 * g + b) * 32 + 4 * j) * 2 + (lane & 7)) * 128), e);
          asm volatile("" ::: "memory");
          const u32 pb0 = WS_P + ((2 * g + b) * 32 + 4 * j) * 2048;
          u32 spins = 0;
          do {
            load4x2_sc1(Pf, lane * 16, rWs, pb0 + (hrot & 3) * 2048, pb0 + ((hrot + 1) & 3) * 2048, pb0 + ((hrot + 2) & 3) * 2048,
                        pb0 + ((hrot + 3) & 3) * 2048);
          } while (!all_written(Pf) && ++spins < SPIN_CAP);
        }
      }
#pragma unroll
      for (int k = 0; k < 40; ++k)
        if (k >= pvsched(n) && k < pvsched(n + 1)) pvi(k);
      SB();
    });
    {
      const u32 pos = (r16 >> 1) * 8 + (r16 & 1) * 4 + g4;
      const u32 sb = WS_SLAB + (w * 4 + cw) * 5120;
#pragma unroll
      for (int cb = 0; cb < 5; ++cb) {
        v4u v;
        v[0] = pk2(O[cb][0][0], O[cb][0][1]); v[1] = pk2(O[cb][0][2], O[cb][0][3]);
        v[2] = pk2(O[cb][1][0], O[cb][1][1]); v[3] = pk2(O[cb][1][2], O[cb][1][3]);
        __builtin_amdgcn_raw_buffer_store_b128(v, rWs, pos * 16 + 1024 * cb, sb, 16  );
      }
      wait_vm<0>();
      if (lane == 0) __hip_atomic_store((GAS u32*)(ws + WS_FLAGO + (w * 4 + cw) * 128), e, RLX_AGENT);
    }
    {
      const int w0 = w & ~7;
      const int cb = lane < 40 ? lane >> 3 : 4, rr = lane & 7;
      poll_eq((GAS u32*)(ws + WS_FLAGO + ((w0 + (lane & 7)) * 4 + cw) * 128), e);
      asm volatile("" ::: "memory");
      v4u v[8];
      load8_sc1(v, (8 * j + rr) * 16 + 1024 * cb, rWs, WS_SLAB + (w0 * 4 + cw) * 5120, 4 * 5120);
      if (lane < 40) {
        float a[8];
#pragma unroll
        for (int d = 0; d < 4; ++d) {
          float lo[8], hi[8];
#pragma unroll
          for (int i = 0; i < 8; ++i) {
            lo[i] = __uint_as_float(v[i][d] << 16);
            hi[i] = __uint_as_float(v[i][d] & 0xffff0000u);
          }
          a[2 * d] = ((lo[0] + lo[1]) + (lo[2] + lo[3])) + ((lo[4] + lo[5]) + (lo[6] + lo[7]));
          a[2 * d + 1] = ((hi[0] + hi[1]) + (hi[2] + hi[3])) + ((hi[4] + hi[5]) + (hi[6] + hi[7]));
        }
        const int qp = 2 * j + (rr >> 2), gg = rr & 3;
        const u32 col = 160 * hh + 80 * half + 16 * cb + 4 * gg;
        GAS char* o0 = (GAS char*)out + (size_t)((2 * g + b) * 32 + qp) * 1280 + col * 2;
        *(GAS v2u*)o0 = (v2u){pk2(a[0], a[1]), pk2(a[2], a[3])};
        *(GAS v2u*)(o0 + 16 * 1280) = (v2u){pk2(a[4], a[5]), pk2(a[6], a[7])};
      }
      if (j == 0 && hh == 0 && cw == 0 && lane == 0) __hip_atomic_store((GAS u32*)(ws + WS_E + g * 128), e, RLX_AGENT);
    }
    {
      const unsigned long long tend = __builtin_amdgcn_s_memrealtime();
      unsigned long long mn = ~0ull;
      for (int i = 0; i < 4; ++i) {
        unsigned long long v2 = __hip_atomic_load((GAS unsigned long long*)(ws + (20u << 20) + (i * 64 + lane) * 8), RLX_AGENT);
        mn = v2 < mn ? v2 : mn;
      }
      for (int o = 32; o >= 1; o >>= 1) { unsigned long long a = __shfl_xor(mn, o); mn = a < mn ? a : mn; }
      const unsigned long long d = (tend - mn) * 100;
      const unsigned long long t1 = __builtin_amdgcn_s_memrealtime();
      if (d < 2000000ull) while (__builtin_amdgcn_s_memrealtime() - t1 < d) __builtin_amdgcn_s_sleep(8);
    }
  };

  if (wave < 4) compute(); else loader();
}

extern "C" void kernel_launch(void* const* d_in, const int* in_sizes, int n_in, void* d_out, int out_size, void* d_ws, size_t ws_size,
                              hipStream_t stream) {
  (void)hipFuncSetAttribute((const void*)gqa_block_kernel, hipFuncAttributeMaxDynamicSharedMemorySize, LDS_BYTES);
  gqa_block_kernel<<<256, 512, LDS_BYTES, stream>>>((const char*)d_in[0], (const char*)d_in[1], (const char*)d_in[2], (const char*)d_in[3],
                                                     (const char*)d_in[4], (char*)d_out, (char*)d_ws);
}
```

```cpp
#include <hip/hip_runtime.h>
#include <hip/hip_bf16.h>
#include <stdint.h>

typedef unsigned u32;
typedef u32 v4u __attribute__((ext_vector_type(4)));
typedef u32 v2u __attribute__((ext_vector_type(2)));
typedef float f32x2 __attribute__((ext_vector_type(2)));
typedef float f32x4 __attribute__((ext_vector_type(4)));
typedef short s16x4 __attribute__((ext_vector_type(4)));
typedef __bf16 bf16x2 __attribute__((ext_vector_type(2)));
typedef __bf16 bf16x8 __attribute__((ext_vector_type(8)));
#define LAS __attribute__((address_space(3)))
#define GAS __attribute__((address_space(1)))
#define DI __device__ __forceinline__
#define RLX_AGENT __ATOMIC_RELAXED, __HIP_MEMORY_SCOPE_AGENT
#define SB() __builtin_amdgcn_sched_barrier(0)

namespace {
constexpr int SLOT = 32768, ACT = 24576, NSLOT = 4, KREG = 0, SCR = 24576, RING = 32768, LDS_BYTES = RING + SLOT * NSLOT;
constexpr int NST = 22;
constexpr u32 WS_E = 0, WS_FLAGP = 4096, WS_FLAGO = 135168, WS_P = 266240, WS_SLAB = 1314816;
constexpr u32 WS_USED = WS_SLAB + 256 * 20480;
constexpr u32 SPIN_CAP = 1u << 18;
constexpr int LA = 8;
constexpr int EPO_N = 4;
constexpr int FDMA_S = 10;
constexpr int PDMA_S = 15;
constexpr int PCHK_N = 14;

template <bool SC1>
DI void dma(u32 voff, __amdgpu_buffer_rsrc_t rs, u32 soff, u32 ldsdst) {
  if constexpr (SC1)
    asm volatile("s_mov_b32 m0, %2\n\ts_nop 0\n\tbuffer_load_dwordx4 %0, %1, %3 offen sc1 lds" ::"v"(voff), "s"(rs), "s"(ldsdst), "s"(soff) : "memory");
  else
    asm volatile("s_mov_b32 m0, %2\n\ts_nop 0\n\tbuffer_load_dwordx4 %0, %1, %3 offen lds" ::"v"(voff), "s"(rs), "s"(ldsdst), "s"(soff) : "memory");
}
DI void dma_word(u32 voff, __amdgpu_buffer_rsrc_t rs, u32 soff, u32 ldsdst) {
  asm volatile("s_mov_b32 m0, %2\n\ts_nop 0\n\tbuffer_load_dword %0, %1, %3 offen sc1 lds" ::"v"(voff), "s"(rs), "s"(ldsdst), "s"(soff) : "memory");
}
template <int N> DI void wait_vm() { asm volatile("s_waitcnt vmcnt(%0)" ::"i"(N) : "memory"); }
DI void bar() {
  SB();
  asm volatile("s_barrier" ::: "memory");
  SB();
}
DI void wait_lgkm0() {
  asm volatile("" ::: "memory");
  __builtin_amdgcn_s_waitcnt(0xC07F);
  asm volatile("" ::: "memory");
}
DI u32 rfl(u32 v) { return __builtin_amdgcn_readfirstlane(v); }
DI u32 pk2(float a, float b) {
  f32x2 v = {a, b};
  return __builtin_bit_cast(u32, __builtin_convertvector(v, bf16x2));
}
DI LAS char* lp(u32 a) { return (LAS char*)a; }
DI bf16x8 lds128(u32 a, int off) { return *(LAS bf16x8*)(lp(a) + off); }
DI s16x4 ldstr4(u32 a, int off) { return __builtin_amdgcn_ds_read_tr16_b64_v4i16((LAS s16x4*)(lp(a) + off)); }
DI bf16x8 cat8(s16x4 a, s16x4 b) { return __builtin_bit_cast(bf16x8, __builtin_shufflevector(a, b, 0, 1, 2, 3, 4, 5, 6, 7)); }
DI f32x4 mfma16(bf16x8 a, bf16x8 b, f32x4 c) { return __builtin_amdgcn_mfma_f32_16x16x32_bf16(a, b, c, 0, 0, 0); }

template <int V> struct IC { static constexpr int value = V; };
template <int I, int N, class F> DI void static_for(F&& f) {
  if constexpr (I < N) {
    f(IC<I>{});
    static_for<I + 1, N>(f);
  }
}
constexpr int rsched1(int ks, int m) { return ks == 0 ? (m <= 7 ? 2 * m : 14) : (m == 0 ? 0 : (m <= 4 ? m + 1 : (m <= 12 ? m + 2 : 14))); }
constexpr int rsched2(int ks, int m) { return ks == 0 ? (m <= 4 ? 2 * m : (m <= 8 ? m + 4 : 12)) : (m == 0 ? 0 : (m <= 4 ? m + 1 : m + 2)); }
constexpr int pvsched(int n) {
  if (n <= PCHK_N) return 0;
  if (n <= 18) { const int a = (20 * (n - PCHK_N - 1) + (17 - PCHK_N)) / (17 - PCHK_N); return a > 20 ? 20 : a; }
  if (n <= 23) return 20 + 2 * (n - 18);
  return 40;
}
static_assert(pvsched(18) == 20 && pvsched(23) == 30 && pvsched(24) == 40 && PCHK_N >= 11 && PCHK_N <= 16, "");
static_assert(2 * (PDMA_S + 1 - 10) - 1 <= PCHK_N - 1 && FDMA_S < PDMA_S && FDMA_S >= 10, "flags before fragments, both landed before they are read");

DI void store16_sc1(v4u v, __amdgpu_buffer_rsrc_t rs, u32 vo, u32 so) {
  __builtin_amdgcn_raw_buffer_store_b128(v, rs, vo, so, 16  );
  SB();
  asm volatile("s_nop 1");
  SB();
}
DI void load8_sc1(v4u (&v)[8], u32 vo, __amdgpu_buffer_rsrc_t rs, u32 s0, u32 step) {
  u32 s1 = s0 + step, s2 = s1 + step, s3 = s2 + step, s4 = s3 + step, s5 = s4 + step, s6 = s5 + step, s7 = s6 + step;
  asm volatile(
      "s_nop 4\n\t"
      "buffer_load_dwordx4 %0, %8, %9, %10 offen sc1\n\t"
      "buffer_load_dwordx4 %1, %8, %9, %11 offen sc1\n\t"
      "buffer_load_dwordx4 %2, %8, %9, %12 offen sc1\n\t"
      "buffer_load_dwordx4 %3, %8, %9, %13 offen sc1\n\t"
      "buffer_load_dwordx4 %4, %8, %9, %14 offen sc1\n\t"
      "buffer_load_dwordx4 %5, %8, %9, %15 offen sc1\n\t"
      "buffer_load_dwordx4 %6, %8, %9, %16 offen sc1\n\t"
      "buffer_load_dwordx4 %7, %8, %9, %17 offen sc1\n\t"
      "s_waitcnt vmcnt(0)"
      : "=&v"(v[0]), "=&v"(v[1]), "=&v"(v[2]), "=&v"(v[3]), "=&v"(v[4]), "=&v"(v[5]), "=&v"(v[6]), "=&v"(v[7])
      : "v"(vo), "s"(rs), "s"(s0), "s"(s1), "s"(s2), "s"(s3), "s"(s4), "s"(s5), "s"(s6), "s"(s7)
      : "memory");
}
DI void load4x2_sc1(v4u (&v)[8], u32 vo, __amdgpu_buffer_rsrc_t rs, u32 s0, u32 s1, u32 s2, u32 s3) {
  asm volatile(
      "s_nop 4\n\t"
      "buffer_load_dwordx4 %0, %8, %9, %10 offen sc1\n\t"
      "buffer_load_dwordx4 %1, %8, %9, %10 offen offset:1024 sc1\n\t"
      "buffer_load_dwordx4 %2, %8, %9, %11 offen sc1\n\t"
      "buffer_load_dwordx4 %3, %8, %9, %11 offen offset:1024 sc1\n\t"
      "buffer_load_dwordx4 %4, %8, %9, %12 offen sc1\n\t"
      "buffer_load_dwordx4 %5, %8, %9, %12 offen offset:1024 sc1\n\t"
      "buffer_load_dwordx4 %6, %8, %9, %13 offen sc1\n\t"
      "buffer_load_dwordx4 %7, %8, %9, %13 offen offset:1024 sc1\n\t"
      "s_waitcnt vmcnt(0)"
      : "=&v"(v[0]), "=&v"(v[1]), "=&v"(v[2]), "=&v"(v[3]), "=&v"(v[4]), "=&v"(v[5]), "=&v"(v[6]), "=&v"(v[7])
      : "v"(vo), "s"(rs), "s"(s0), "s"(s1), "s"(s2), "s"(s3)
      : "memory");
}
DI void poll_eq(GAS u32* p, u32 want) {
  for (u32 spins = 0; spins < SPIN_CAP; ++spins) {
    const u32 v = __hip_atomic_load(p, RLX_AGENT);
    if (__all(v == want)) break;
    __builtin_amdgcn_s_sleep(1);
  }
}
DI bool all_written(const v4u (&v)[8]) {
  u32 mx = 0;
#pragma unroll
  for (int i = 0; i < 8; ++i) {
    const u32 a = v[i][0] > v[i][1] ? v[i][0] : v[i][1], c = v[i][2] > v[i][3] ? v[i][2] : v[i][3];
    const u32 d = a > c ? a : c;
    mx = mx > d ? mx : d;
  }
  return __all(mx != 0xffffffffu);
}
}

__global__ __launch_bounds__(512, 2) void gqa_block_kernel(const char* __restrict__ x, const char* __restrict__ kc, const char* __restrict__ vc,
                                                           const char* __restrict__ wq, const char* __restrict__ wo, char* __restrict__ out,
                                                           char* wsp) {
  extern __shared__ __attribute__((aligned(16))) char smem_[];
  const int w = blockIdx.x;
  const int wave = rfl(threadIdx.x >> 6);
  const int lane = threadIdx.x & 63;
  const int j = w & 7, g = w >> 5, hh = (w >> 3) & 3;
  const int h = 4 * j + hh;
  const int hrot = hh + g;
  GAS char* ws = (GAS char*)wsp;
  const u32 lbase = (u32)(size_t)(LAS char*)smem_;

  {
    const unsigned long long tstart = __builtin_amdgcn_s_memrealtime();
    if (threadIdx.x == 0) __hip_atomic_store((GAS unsigned long long*)(ws + (20u << 20) + w * 8), tstart, RLX_AGENT);
  }
  auto loader = [&]() __attribute__((always_inline)) {
    const int lw = wave - 4;
    __builtin_amdgcn_s_setprio(1);
    const __amdgpu_buffer_rsrc_t rX = __builtin_amdgcn_make_buffer_rsrc((void*)x, 0, 512 * 1280, 0x00020000);
    const __amdgpu_buffer_rsrc_t rWq = __builtin_amdgcn_make_buffer_rsrc((void*)wq, 0, 640 * 12288, 0x00020000);
    u32 ra[2], ca[2], vA[2], vKV[2];
#pragma unroll
    for (int i = 0; i < 2; ++i) {
      ra[i] = 8 * i + (lane >> 3);
      ca[i] = 16 * ((lane & 7) ^ ((ra[i] >> 1) & 7));
      vA[i] = ra[i] * 1280 + ca[i];
    }
    u32 kk[6], cc[6], vW[6];
#pragma unroll
    for (int i = 0; i < 6; ++i) {
      const u32 L = 8 * i + (lane >> 3), k = (L * 43) >> 7;
      kk[i] = k;
      cc[i] = 128 * (L - 3 * k) + 16 * ((lane & 7) ^ ((k & 2) << 1) ^ ((k & 8) >> 2));
      vW[i] = k * 12288 + cc[i];
    }
#pragma unroll
    for (int i = 0; i < 2; ++i) vKV[i] = ra[i] * 384 + ca[i];
    const u32 ldsW = lbase + RING + lw * 6144, ldsA = lbase + RING + ACT + lw * 2048;
    const u32 soW0 = h * 384 + 16 * lw * 12288, soA0 = (64 * g + 16 * lw) * 1280;
    const int bl = 2 * g + (lw >> 1);
    const u32 soKV0 = (u32)(bl * 32 * 32 + 16 * (lw & 1)) * 384;
    const u32 L0 = (5 * hh) >> 1;
    int kcur = (g + 3 * hh) % 10;
    const int crot = g >> 2;
    const int kpos = ((w >> 3) + j) & 7;
    const __amdgpu_buffer_rsrc_t rWs = __builtin_amdgcn_make_buffer_rsrc((void*)wsp, 0, WS_USED, 0x00020000);
    u32 Ev = 0;
    auto after_barrier = [&](int s) {
      if (s == 0) {
        if (lw == 0) dma_word(0, rWs, WS_E + g * 128, lbase + SCR);
        asm volatile("buffer_load_dword %0, %1, %2, %3 offen sc1" : "+v"(Ev) : "v"(0u), "s"(rWs), "s"(WS_E + g * 128) : "memory");
        const v4u ones = {~0u, ~0u, ~0u, ~0u};
        asm volatile("buffer_store_dwordx4 %0, %1, %2, %3 offen sc1\n\ts_nop 1" ::"v"(ones), "v"(lane * 16 + 1024 * (lw & 1)), "s"(rWs),
                     "s"(WS_P + (bl * 32 + h) * 2048)
                     : "memory");
      }
      if (s == 1) {
        asm volatile("" : "+v"(Ev));
        const u32 ev = Ev + 1;
        if (lane == 0)
          asm volatile("buffer_store_dword %0, %1, %2, %3 offen sc1\n\ts_nop 1" ::"v"(ev), "v"(0u), "s"(rWs),
                       "s"(WS_FLAGP + ((bl * 32 + h) * 2 + (lw & 1)) * 128)
                       : "memory");
      }
      if (s == FDMA_S && lw == 0)
        dma_word((((lane >> 3) & 1) * 64 + (lane & 7)) * 128, rWs, WS_FLAGP + ((2 * g * 32 + 4 * j) * 2) * 128, lbase + KREG + 16384);
      if (s == PDMA_S) {
#pragma unroll
        for (int i = 0; i < 2; ++i)
#pragma unroll
          for (int qb = 0; qb < 2; ++qb) {
            const int hx = 2 * (lw & 1) + i;
            dma<true>(lane * 16, rWs, WS_P + (bl * 32 + 4 * j + ((hrot + hx) & 3)) * 2048 + 1024 * qb,
                      lbase + KREG + (((lw >> 1) * 4 + hx) * 2 + qb) * 1024);
          }
      }
      if (s < 8 && s == kpos) {
        const __amdgpu_buffer_rsrc_t rK = __builtin_amdgcn_make_buffer_rsrc((void*)kc, 0, 512 * 12288, 0x00020000);
#pragma unroll
        for (int c = 0; c < 3; ++c)
#pragma unroll
          for (int i = 0; i < 2; ++i) dma<false>(vKV[i], rK, soKV0 + h * 12288 + 128 * c, lbase + KREG + 8192 * c + lw * 2048 + 1024 * i);
      }
    };
    static_for<0, NST>([&](auto tc_) {
      constexpr int t = decltype(tc_)::value;
      constexpr u32 slot = (t % NSLOT) * SLOT;
      u32 soW, soA;
      if constexpr (t < 10) {
        soW = soW0 + kcur * (64 * 12288);
        soA = soA0 + kcur * 128;
        kcur = kcur == 9 ? 0 : kcur + 1;
      } else {
        if constexpr (t == 10) {
#pragma unroll
          for (int i = 0; i < 2; ++i) vA[i] = vKV[i];
#pragma unroll
          for (int i = 0; i < 6; ++i) vW[i] = kk[i] * 1280 + cc[i];
        }
        constexpr int hi = (t - 10) / 3, c0 = (t - 10) % 3;
        const int c = c0 + crot >= 3 ? c0 + crot - 3 : c0 + crot;
        const u32 hp = 4 * j + ((hrot + hi) & 3);
        soW = (hp * 192 + 64 * c + 16 * lw) * 1280 + 128 * L0;
        soA = soKV0 + hp * 12288 + 128 * c;
      }
      static_for<0, 8>([&](auto ic_) {
        constexpr int i = decltype(ic_)::value, p = 8 * t + i;
        if constexpr (t < 10) {
          if constexpr (i < 2) dma<false>(vA[i], rX, soA, slot + ldsA + 1024 * i);
          else dma<false>(vW[i - 2], rWq, soW, slot + ldsW + 1024 * (i - 2));
        } else {
          const __amdgpu_buffer_rsrc_t rV = __builtin_amdgcn_make_buffer_rsrc((void*)vc, 0, 512 * 12288, 0x00020000);
          const __amdgpu_buffer_rsrc_t rWo = __builtin_amdgcn_make_buffer_rsrc((void*)wo, 0, 6144 * 1280, 0x00020000);
          if constexpr (i < 2) dma<false>(vA[i], rV, soA, slot + ldsA + 1024 * i);
          else dma<false>(vW[i - 2], rWo, soW, slot + ldsW + 1024 * (i - 2));
        }
        if constexpr (p + 1 - LA >= 8 && (p + 1 - LA) % 8 == 0) {
          constexpr int s = (p + 1 - LA) / 8 - 1;
          wait_vm<LA>();
          bar();
          if constexpr (s < 8 || s == FDMA_S || s == PDMA_S) after_barrier(s);
        }
      });
    });
    static_for<(8 * NST - LA) / 8, NST>([&](auto sc_) {
      constexpr int s = decltype(sc_)::value;
      wait_vm<8 * (NST - 1 - s)>();
      bar();
    });
  };

  auto compute = [&]() __attribute__((always_inline)) {
    const int cw = wave, b = cw >> 1, half = cw & 1;
    const __amdgpu_buffer_rsrc_t rWs = __builtin_amdgcn_make_buffer_rsrc((void*)wsp, 0, WS_USED, 0x00020000);
    const int r16 = lane & 15, g4 = lane >> 4, q4 = (lane & 15) >> 2, p4 = lane & 3;
    const int x2 = (g4 & 1) | ((q4 >> 1) << 1);
    const u32 fsw = (r16 >> 1) & 7;
    u32 e = 0, Ev = 0;
    f32x4 accS[2][2];
    {
      f32x4 accQ[6][2];
      s16x4 kl[3][2], kh[3][2];
      u32 aW[6], aX[2], aY[2], aK[3][2];
#pragma unroll
      for (int tb = 0; tb < 6; ++tb) aW[tb] = lbase + RING + 384 * (8 * g4 + q4) + 32 * ((6 * half + tb) ^ x2) + 16 * (p4 >> 1) + 8 * (p4 & 1);
#pragma unroll
      for (int ks = 0; ks < 2; ++ks) {
        aX[ks] = lbase + RING + ACT + 128 * (32 * b + 16 * half + r16) + 16 * ((4 * ks + g4) ^ fsw);
        aY[ks] = lbase + RING + ACT + 128 * (32 * b + 16 * (half ^ 1) + r16) + 16 * ((4 * ks + g4) ^ fsw);
      }
#pragma unroll
      for (int u = 0; u < 3; ++u)
#pragma unroll
        for (int a = 0; a < 2; ++a) {
          const int ch = 12 * half + 4 * u + 2 * a + (g4 >> 1);
          aK[u][a] = lbase + KREG + 8192 * (ch >> 3) + 128 * (32 * b + r16) + 16 * ((ch & 7) ^ fsw) + 8 * (g4 & 1);
        }
#pragma unroll
      for (int i = 0; i < 6; ++i) asm volatile("" : "+v"(aW[i]));
#pragma unroll
      for (int i = 0; i < 2; ++i) asm volatile("" : "+v"(aX[i]), "+v"(aY[i]));
#pragma unroll
      for (int i = 0; i < 6; ++i) asm volatile("" : "+v"(aK[i >> 1][i & 1]));

      s16x4 wl[2][6], wh[2][6];
      bf16x8 xf[2][2];
      auto rd1 = [&](int n, int part) {
        const int set = n & 1, ks = n & 1;
        if (part == 0) xf[set][0] = lds128(aX[ks], 0);
        else if (part == 3) xf[set][1] = lds128(aY[ks], 0);
        else {
          const int q = part < 3 ? part - 1 : part - 2, tb = q >> 1;
          if (q & 1) wh[set][tb] = ldstr4(aW[tb], 384 * (32 * ks + 4));
          else wl[set][tb] = ldstr4(aW[tb], 384 * 32 * ks);
        }
      };
      auto rdk = [&](int m) {
        const int u = m >> 2, kb = (m >> 1) & 1, a = m & 1;
        const s16x4 v = *(LAS s16x4*)(lp(aK[u][a]) + 2048 * kb);
        if (a) kh[u][kb] = v; else kl[u][kb] = v;
      };
      auto adv1 = [&](int s) {
        const u32 d = (s % NSLOT == 0) ? (u32)(-(NSLOT - 1) * SLOT) : (u32)SLOT;
#pragma unroll
        for (int i = 0; i < 6; ++i) { aW[i] += d; asm volatile("" : "+v"(aW[i])); }
#pragma unroll
        for (int i = 0; i < 2; ++i) { aX[i] += d; aY[i] += d; asm volatile("" : "+v"(aX[i]), "+v"(aY[i])); }
      };
      auto mm1 = [&](int n, int tb, int qb) {
        const int set = n & 1;
        const f32x4 c0 = (n == 0) ? (f32x4){0.f, 0.f, 0.f, 0.f} : accQ[tb][qb];
        accQ[tb][qb] = mfma16(cat8(wl[set][tb], wh[set][tb]), xf[set][qb], c0);
      };
      bar();
#pragma unroll
      for (int q = 0; q < 14; ++q) rd1(0, q);
      SB();
#pragma unroll
      for (int n = 0; n < 19; ++n) {
        const int s = n >> 1, ks = n & 1;
        if (ks == 1) {
          wait_lgkm0();
          bar();
        }
#pragma unroll
        for (int m = 0; m < 12; ++m) {
          mm1(n, m >> 1, m & 1);
          SB();
#pragma unroll
          for (int q = 0; q < 14; ++q)
            if (q >= rsched1(ks, m) && q < rsched1(ks, m + 1)) rd1(n + 1, q);
          if (ks == 0 && s < 9 && m == 9) adv1(s + 1);
          SB();
        }
        if (n == EPO_N - 2) Ev = *(LAS u32*)(lp(lbase + SCR));
        if (n == EPO_N) {
          e = rfl(Ev) + 1;
          asm volatile("" : "+s"(e));
        }
        SB();
      }
      v4u qp[2];
      auto qpack = [&](int u, int qb) {
        qp[qb][0] = pk2(accQ[2 * u][qb][0], accQ[2 * u][qb][1]);
        qp[qb][1] = pk2(accQ[2 * u][qb][2], accQ[2 * u][qb][3]);
        qp[qb][2] = pk2(accQ[2 * u + 1][qb][0], accQ[2 * u + 1][qb][1]);
        qp[qb][3] = pk2(accQ[2 * u + 1][qb][2], accQ[2 * u + 1][qb][3]);
      };
      auto smm = [&](int u) {
#pragma unroll
        for (int qb = 1; qb >= 0; --qb)
#pragma unroll
          for (int kb = 0; kb < 2; ++kb) {
            const f32x4 c0 = u == 0 ? (f32x4){0.f, 0.f, 0.f, 0.f} : accS[kb][qb];
            accS[kb][qb] = mfma16(cat8(kl[u][kb], kh[u][kb]), __builtin_bit_cast(bf16x8, qp[qb]), c0);
          }
      };
#pragma unroll
      for (int m = 0; m < 12; ++m) {
        mm1(19, m >> 1, m & 1);
        SB();
        if (m < 6) { rdk(2 * m); rdk(2 * m + 1); }
        if (m == 5) qpack(0, 0);
        if (m == 6) qpack(0, 1);
        if (m == 7) smm(0);
        if (m == 9) qpack(1, 0);
        if (m == 10) qpack(1, 1);
        if (m == 11) smm(1);
        SB();
      }
      qpack(2, 0);
      qpack(2, 1);
      smm(2);
      const u32 scw = lbase + SCR + 4096 * b + 2048 * half + 16 * lane;
#pragma unroll
      for (int kb = 0; kb < 2; ++kb) *(LAS f32x4*)(lp(scw) + 1024 * kb) = accS[kb][1];
      wait_lgkm0();
    }
    const int c016 = (hh & 1) * 2;
    u32 aW[5], aV[2];
#pragma unroll
    for (int cb = 0; cb < 5; ++cb)
      aW[cb] = lbase + RING + SLOT * (10 % NSLOT) + 384 * (8 * g4 + q4) + 32 * ((c016 + 5 * half + cb) ^ x2) + 16 * (p4 >> 1) + 8 * (p4 & 1);
#pragma unroll
    for (int ks = 0; ks < 2; ++ks) aV[ks] = lbase + RING + SLOT * (10 % NSLOT) + ACT + 128 * (32 * b + r16) + 16 * ((4 * ks + g4) ^ fsw);
#pragma unroll
    for (int i = 0; i < 5; ++i) asm volatile("" : "+v"(aW[i]));
#pragma unroll
    for (int i = 0; i < 2; ++i) asm volatile("" : "+v"(aV[i]));
    bar();
    s16x4 wl[2][5], wh[2][5];
    bf16x8 vf[2][2];
    auto rd2 = [&](int n, int part) {
      const int set = n & 1, ks = n & 1;
      if (part == 0) vf[set][0] = lds128(aV[ks], 0);
      else if (part == 3) vf[set][1] = lds128(aV[ks], 2048);
      else {
        const int q = part < 3 ? part - 1 : part - 2, cb = q >> 1;
        if (q & 1) wh[set][cb] = ldstr4(aW[cb], 384 * (32 * ks + 4));
        else wl[set][cb] = ldstr4(aW[cb], 384 * 32 * ks);
      }
    };
    auto adv2 = [&](int s) {
      const u32 d = (s % NSLOT == 0) ? (u32)(-(NSLOT - 1) * SLOT) : (u32)SLOT;
#pragma unroll
      for (int i = 0; i < 5; ++i) { aW[i] += d; asm volatile("" : "+v"(aW[i])); }
#pragma unroll
      for (int i = 0; i < 2; ++i) { aV[i] += d; asm volatile("" : "+v"(aV[i])); }
    };
    f32x4 X[2][5];
    v4u Xp[4][5];
    v4u Pf[8];
    f32x4 O[5][2];
#pragma unroll
    for (int cb = 0; cb < 5; ++cb)
#pragma unroll
      for (int qb = 0; qb < 2; ++qb) O[cb][qb] = (f32x4){0.f, 0.f, 0.f, 0.f};
    auto mm2 = [&](int n, int cb, int rb) {
      const int set = n & 1;
      const f32x4 c0 = (n % 6 == 0) ? (f32x4){0.f, 0.f, 0.f, 0.f} : X[rb][cb];
      X[rb][cb] = mfma16(vf[set][rb], cat8(wl[set][cb], wh[set][cb]), c0);
    };
    auto pack = [&](int hi, int cb) {
      Xp[hi][cb][0] = pk2(X[0][cb][0], X[0][cb][1]);
      Xp[hi][cb][1] = pk2(X[0][cb][2], X[0][cb][3]);
      Xp[hi][cb][2] = pk2(X[1][cb][0], X[1][cb][1]);
      Xp[hi][cb][3] = pk2(X[1][cb][2], X[1][cb][3]);
      asm volatile("" : "+v"(Xp[hi][cb]));
    };
    auto pvi = [&](int k) {
      const int hi = k / 10, cb = (k % 10) >> 1, qb = k & 1;
      O[cb][qb] = mfma16(__builtin_bit_cast(bf16x8, Xp[hi][cb]), __builtin_bit_cast(bf16x8, Pf[2 * hi + qb]), O[cb][qb]);
    };
    float sv[8], mc = 0.f, sum = 0.f, t0 = 0.f, t1 = 0.f;
    v4u pfo;
    constexpr float scale = 0.07216878364870322f, cs = 1.4426950408889634f;
    {
      const u32 scr = lbase + SCR + 4096 * b + 2048 * (half ^ 1) + 16 * lane;
      const f32x4 o0 = *(LAS f32x4*)(lp(scr)), o1 = *(LAS f32x4*)(lp(scr) + 1024);
      SB();
#pragma unroll
      for (int q = 0; q < 12; ++q) rd2(0, q);
      SB();
#pragma unroll
      for (int r = 0; r < 4; ++r) {
        sv[r] = accS[0][0][r] + o0[r];
        sv[4 + r] = accS[1][0][r] + o1[r];
      }
#pragma unroll
      for (int d = 0; d < 4; ++d) {
        const u32 a = pk2(sv[2 * d], sv[2 * d + 1]);
        const u32 c = pk2(__uint_as_float(a << 16) * scale, __uint_as_float(a & 0xffff0000u) * scale);
        sv[2 * d] = __uint_as_float(c << 16);
        sv[2 * d + 1] = __uint_as_float(c & 0xffff0000u);
      }
      float m = fmaxf(fmaxf(fmaxf(sv[0], sv[1]), fmaxf(sv[2], sv[3])), fmaxf(fmaxf(sv[4], sv[5]), fmaxf(sv[6], sv[7])));
      const auto s1 = __builtin_amdgcn_permlane16_swap(__float_as_uint(m), __float_as_uint(m), false, false);
      m = fmaxf(__uint_as_float(s1[0]), __uint_as_float(s1[1]));
      const auto s2 = __builtin_amdgcn_permlane32_swap(__float_as_uint(m), __float_as_uint(m), false, false);
      m = fmaxf(__uint_as_float(s2[0]), __uint_as_float(s2[1]));
      mc = -m * cs;
    }
    SB();
    auto smx = [&](int n, int m) {
      if (n == 0) {
        if (m == 0 || m == 1) {
#pragma unroll
          for (int r = 0; r < 4; ++r) sv[4 * m + r] = __builtin_fmaf(sv[4 * m + r], cs, mc);
        } else if (m <= 5) {
          sv[2 * (m - 2)] = __builtin_amdgcn_exp2f(sv[2 * (m - 2)]);
          sv[2 * (m - 2) + 1] = __builtin_amdgcn_exp2f(sv[2 * (m - 2) + 1]);
        } else if (m == 6) {
          t0 = (sv[0] + sv[1]) + (sv[2] + sv[3]);
        } else if (m == 7) {
          t1 = (sv[4] + sv[5]) + (sv[6] + sv[7]);
          sum = t0 + t1;
        } else if (m == 8) {
          const auto s1 = __builtin_amdgcn_permlane16_swap(__float_as_uint(sum), __float_as_uint(sum), false, false);
          sum = __uint_as_float(s1[0]) + __uint_as_float(s1[1]);
        } else {
          const auto s2 = __builtin_amdgcn_permlane32_swap(__float_as_uint(sum), __float_as_uint(sum), false, false);
          sum = __builtin_amdgcn_rcpf(__uint_as_float(s2[0]) + __uint_as_float(s2[1]));
        }
      } else if (n == 1) {
        if (m == 1 || m == 2) {
#pragma unroll
          for (int r = 0; r < 4; ++r) sv[4 * (m - 1) + r] *= sum;
        } else if (m == 3) {
#pragma unroll
          for (int d = 0; d < 4; ++d) pfo[d] = pk2(sv[2 * d], sv[2 * d + 1]);
        } else if (m == 4) {
#pragma unroll
          for (int d = 0; d < 4; ++d) pfo[d] = pfo[d] < 0xfffffffeu ? pfo[d] : 0xfffffffeu;
        } else if (m == 5) {
          store16_sc1(pfo, rWs, lane * 16 + 1024 * half, WS_P + ((2 * g + b) * 32 + h) * 2048);
        }
      }
    };
    u32 fval = 0;
    static_for<0, 24>([&](auto nc_) {
      constexpr int n = decltype(nc_)::value;
      constexpr int s = 10 + (n >> 1), ks = n & 1, hi = n / 6;
      constexpr bool last = (n % 6 == 5);
      if (ks == 1 && s < NST - 1) {
        wait_lgkm0();
        bar();
      }
#pragma unroll
      for (int m = 0; m < 10; ++m) {
        mm2(n, m >> 1, m & 1);
        SB();
        if (n + 1 < 24) {
#pragma unroll
          for (int q = 0; q < 12; ++q)
            if (q >= rsched2(ks, m) && q < rsched2(ks, m + 1)) rd2(n + 1, q);
        }
        if (n < 2) smx(n, m);
        if (last) {
          if (m == 4) pack(hi, 0);
          if (m == 6) pack(hi, 1);
          if (m == 8) pack(hi, 2);
          if (m == 9) { pack(hi, 3); pack(hi, 4); }
        } else if (ks == 0 && s < NST - 1 && m == 8) adv2(s + 1);
        SB();
      }
      if (n == PCHK_N - 1) {
        fval = *(LAS u32*)(lp(lbase + KREG + 16384 + 4 * (8 * b + (lane & 7))));
#pragma unroll
        for (int i = 0; i < 8; ++i) Pf[i] = *(LAS v4u*)(lp(lbase + KREG + 16 * lane) + (b * 8 + i) * 1024);
      }
      if (n == PCHK_N) {
        asm volatile("" : "+v"(X[0][0]), "+v"(X[0][1]), "+v"(X[0][2]), "+v"(X[0][3]), "+v"(X[0][4]), "+v"(X[1][0]), "+v"(X[1][1]), "+v"(X[1][2]),
                     "+v"(X[1][3]), "+v"(X[1][4]));
        if (!(__all(fval == e) && all_written(Pf))) {
          poll_eq((GAS u32*)(ws + WS_FLAGP + (((2 * g + b) * 32 + 4 * j) * 2 + (lane & 7)) * 128), e);
          asm volatile("" ::: "memory");
          const u32 pb0 = WS_P + ((2 * g + b) * 32 + 4 * j) * 2048;
          u32 spins = 0;
          do {
            load4x2_sc1(Pf, lane * 16, rWs, pb0 + (hrot & 3) * 2048, pb0 + ((hrot + 1) & 3) * 2048, pb0 + ((hrot + 2) & 3) * 2048,
                        pb0 + ((hrot + 3) & 3) * 2048);
          } while (!all_written(Pf) && ++spins < SPIN_CAP);
        }
      }
#pragma unroll
      for (int k = 0; k < 40; ++k)
        if (k >= pvsched(n) && k < pvsched(n + 1)) pvi(k);
      SB();
    });
    {
      const u32 pos = (r16 >> 1) * 8 + (r16 & 1) * 4 + g4;
      const u32 sb = WS_SLAB + (w * 4 + cw) * 5120;
#pragma unroll
      for (int cb = 0; cb < 5; ++cb) {
        v4u v;
        v[0] = pk2(O[cb][0][0], O[cb][0][1]); v[1] = pk2(O[cb][0][2], O[cb][0][3]);
        v[2] = pk2(O[cb][1][0], O[cb][1][1]); v[3] = pk2(O[cb][1][2], O[cb][1][3]);
        store16_sc1(v, rWs, pos * 16 + 1024 * cb, sb);
      }
      wait_vm<0>();
      if (lane == 0) __hip_atomic_store((GAS u32*)(ws + WS_FLAGO + (w * 4 + cw) * 128), e, RLX_AGENT);
    }
    {
      const int w0 = w & ~7;
      const int cb = lane < 40 ? lane >> 3 : 4, rr = lane & 7;
      poll_eq((GAS u32*)(ws + WS_FLAGO + ((w0 + (lane & 7)) * 4 + cw) * 128), e);
      asm volatile("" ::: "memory");
      v4u v[8];
      load8_sc1(v, (8 * j + rr) * 16 + 1024 * cb, rWs, WS_SLAB + (w0 * 4 + cw) * 5120, 4 * 5120);
      if (lane < 40) {
        float a[8];
#pragma unroll
        for (int d = 0; d < 4; ++d) {
          float lo[8], hi[8];
#pragma unroll
          for (int i = 0; i < 8; ++i) {
            lo[i] = __uint_as_float(v[i][d] << 16);
            hi[i] = __uint_as_float(v[i][d] & 0xffff0000u);
          }
          a[2 * d] = ((lo[0] + lo[1]) + (lo[2] + lo[3])) + ((lo[4] + lo[5]) + (lo[6] + lo[7]));
          a[2 * d + 1] = ((hi[0] + hi[1]) + (hi[2] + hi[3])) + ((hi[4] + hi[5]) + (hi[6] + hi[7]));
        }
        const int qp = 2 * j + (rr >> 2), gg = rr & 3;
        const u32 col = 160 * hh + 80 * half + 16 * cb + 4 * gg;
        GAS char* o0 = (GAS char*)out + (size_t)((2 * g + b) * 32 + qp) * 1280 + col * 2;
        *(GAS v2u*)o0 = (v2u){pk2(a[0], a[1]), pk2(a[2], a[3])};
        *(GAS v2u*)(o0 + 16 * 1280) = (v2u){pk2(a[4], a[5]), pk2(a[6], a[7])};
      }
      if (j == 0 && hh == 0 && cw == 0 && lane == 0) __hip_atomic_store((GAS u32*)(ws + WS_E + g * 128), e, RLX_AGENT);
    }
    {
      const unsigned long long tend = __builtin_amdgcn_s_memrealtime();
      unsigned long long mn = ~0ull;
      for (int i = 0; i < 4; ++i) {
        unsigned long long v2 = __hip_atomic_load((GAS unsigned long long*)(ws + (20u << 20) + (i * 64 + lane) * 8), RLX_AGENT);
        mn = v2 < mn ? v2 : mn;
      }
      for (int o = 32; o >= 1; o >>= 1) { unsigned long long a = __shfl_xor(mn, o); mn = a < mn ? a : mn; }
      const unsigned long long d = (tend - mn) * 100;
      const unsigned long long t1 = __builtin_amdgcn_s_memrealtime();
      if (d < 2000000ull) while (__builtin_amdgcn_s_memrealtime() - t1 < d) __builtin_amdgcn_s_sleep(8);
    }
  };

  if (wave < 4) compute(); else loader();
}

extern "C" void kernel_launch(void* const* d_in, const int* in_sizes, int n_in, void* d_out, int out_size, void* d_ws, size_t ws_size,
                              hipStream_t stream) {
  (void)hipFuncSetAttribute((const void*)gqa_block_kernel, hipFuncAttributeMaxDynamicSharedMemorySize, LDS_BYTES);
  gqa_block_kernel<<<256, 512, LDS_BYTES, stream>>>((const char*)d_in[0], (const char*)d_in[1], (const char*)d_in[2], (const char*)d_in[3],
                                                     (const char*)d_in[4], (char*)d_out, (char*)d_ws);
}
```

```cpp
#include <hip/hip_runtime.h>
#include <hip/hip_bf16.h>
#include <stdint.h>

typedef unsigned u32;
typedef u32 v4u __attribute__((ext_vector_type(4)));
typedef u32 v2u __attribute__((ext_vector_type(2)));
typedef float f32x2 __attribute__((ext_vector_type(2)));
typedef float f32x4 __attribute__((ext_vector_type(4)));
typedef short s16x4 __attribute__((ext_vector_type(4)));
typedef __bf16 bf16x2 __attribute__((ext_vector_type(2)));
typedef __bf16 bf16x8 __attribute__((ext_vector_type(8)));
#define LAS __attribute__((address_space(3)))
#define GAS __attribute__((address_space(1)))
#define DI __device__ __forceinline__
#define RLX_AGENT __ATOMIC_RELAXED, __HIP_MEMORY_SCOPE_AGENT
#define SB() __builtin_amdgcn_sched_barrier(0)

namespace {
constexpr int SLOT = 32768, ACT = 24576, NSLOT = 4, KREG = 0, SCR = 24576, RING = 32768, LDS_BYTES = RING + SLOT * NSLOT;
constexpr int NST = 22;
constexpr u32 WS_E = 0, WS_FLAGP = 4096, WS_FLAGO = 135168, WS_P = 266240, WS_SLAB = 1314816;
constexpr u32 WS_USED = WS_SLAB + 256 * 20480;
constexpr u32 SPIN_CAP = 1u << 18;
constexpr int LA = 8;
constexpr int EPO_N = 4;
constexpr int FDMA_S = 10;
constexpr int PDMA_S = 15;
constexpr int PCHK_N = 14;

template <bool SC1>
DI void dma(u32 voff, __amdgpu_buffer_rsrc_t rs, u32 soff, u32 ldsdst) {
  if constexpr (SC1)
    asm volatile("s_mov_b32 m0, %2\n\ts_nop 0\n\tbuffer_load_dwordx4 %0, %1, %3 offen sc1 lds" ::"v"(voff), "s"(rs), "s"(ldsdst), "s"(soff) : "memory");
  else
    asm volatile("s_mov_b32 m0, %2\n\ts_nop 0\n\tbuffer_load_dwordx4 %0, %1, %3 offen lds" ::"v"(voff), "s"(rs), "s"(ldsdst), "s"(soff) : "memory");
}
DI void dma_word(u32 voff, __amdgpu_buffer_rsrc_t rs, u32 soff, u32 ldsdst) {
  asm volatile("s_mov_b32 m0, %2\n\ts_nop 0\n\tbuffer_load_dword %0, %1, %3 offen sc1 lds" ::"v"(voff), "s"(rs), "s"(ldsdst), "s"(soff) : "memory");
}
template <int N> DI void wait_vm() { asm volatile("s_waitcnt vmcnt(%0)" ::"i"(N) : "memory"); }
DI void bar() {
  SB();
  asm volatile("s_barrier" ::: "memory");
  SB();
}
DI void wait_lgkm0() {
  asm volatile("" ::: "memory");
  __builtin_amdgcn_s_waitcnt(0xC07F);
  asm volatile("" ::: "memory");
}
DI u32 rfl(u32 v) { return __builtin_amdgcn_readfirstlane(v); }
DI u32 pk2(float a, float b) {
  f32x2 v = {a, b};
  return __builtin_bit_cast(u32, __builtin_convertvector(v, bf16x2));
}
DI LAS char* lp(u32 a) { return (LAS char*)a; }
DI bf16x8 lds128(u32 a, int off) { return *(LAS bf16x8*)(lp(a) + off); }
DI s16x4 ldstr4(u32 a, int off) { return __builtin_amdgcn_ds_read_tr16_b64_v4i16((LAS s16x4*)(lp(a) + off)); }
DI bf16x8 cat8(s16x4 a, s16x4 b) { return __builtin_bit_cast(bf16x8, __builtin_shufflevector(a, b, 0, 1, 2, 3, 4, 5, 6, 7)); }
DI f32x4 mfma16(bf16x8 a, bf16x8 b, f32x4 c) { return __builtin_amdgcn_mfma_f32_16x16x32_bf16(a, b, c, 0, 0, 0); }

template <int V> struct IC { static constexpr int value = V; };
template <int I, int N, class F> DI void static_for(F&& f) {
  if constexpr (I < N) {
    f(IC<I>{});
    static_for<I + 1, N>(f);
  }
}
constexpr int rsched1(int ks, int m) { return ks == 0 ? (m <= 7 ? 2 * m : 14) : (m == 0 ? 0 : (m <= 4 ? m + 1 : (m <= 12 ? m + 2 : 14))); }
constexpr int rsched2(int ks, int m) { return ks == 0 ? (m <= 4 ? 2 * m : (m <= 8 ? m + 4 : 12)) : (m == 0 ? 0 : (m <= 4 ? m + 1 : m + 2)); }
constexpr int pvsched(int n) {
  if (n <= PCHK_N) return 0;
  if (n <= 18) { const int a = (20 * (n - PCHK_N - 1) + (17 - PCHK_N)) / (17 - PCHK_N); return a > 20 ? 20 : a; }
  if (n <= 23) return 20 + 2 * (n - 18);
  return 40;
}
static_assert(pvsched(18) == 20 && pvsched(23) == 30 && pvsched(24) == 40 && PCHK_N >= 11 && PCHK_N <= 16, "");
static_assert(2 * (PDMA_S + 1 - 10) - 1 <= PCHK_N - 1 && FDMA_S < PDMA_S && FDMA_S >= 10, "flags before fragments, both landed before they are read");

DI void store16_sc1(v4u v, __amdgpu_buffer_rsrc_t rs, u32 vo, u32 so) {
  __builtin_amdgcn_raw_buffer_store_b128(v, rs, vo, so, 16  );
  SB();
  asm volatile("s_nop 1");
  SB();
}
DI void load8_sc1(v4u (&v)[8], u32 vo, __amdgpu_buffer_rsrc_t rs, u32 s0, u32 step) {
  u32 s1 = s0 + step, s2 = s1 + step, s3 = s2 + step, s4 = s3 + step, s5 = s4 + step, s6 = s5 + step, s7 = s6 + step;
  asm volatile(
      "s_nop 4\n\t"
      "buffer_load_dwordx4 %0, %8, %9, %10 offen sc1\n\t"
      "buffer_load_dwordx4 %1, %8, %9, %11 offen sc1\n\t"
      "buffer_load_dwordx4 %2, %8, %9, %12 offen sc1\n\t"
      "buffer_load_dwordx4 %3, %8, %9, %13 offen sc1\n\t"
      "buffer_load_dwordx4 %4, %8, %9, %14 offen sc1\n\t"
      "buffer_load_dwordx4 %5, %8, %9, %15 offen sc1\n\t"
      "buffer_load_dwordx4 %6, %8, %9, %16 offen sc1\n\t"
      "buffer_load_dwordx4 %7, %8, %9, %17 offen sc1\n\t"
      "s_waitcnt vmcnt(0)"
      : "=&v"(v[0]), "=&v"(v[1]), "=&v"(v[2]), "=&v"(v[3]), "=&v"(v[4]), "=&v"(v[5]), "=&v"(v[6]), "=&v"(v[7])
      : "v"(vo), "s"(rs), "s"(s0), "s"(s1), "s"(s2), "s"(s3), "s"(s4), "s"(s5), "s"(s6), "s"(s7)
      : "memory");
}
DI void load4x2_sc1(v4u (&v)[8], u32 vo, __amdgpu_buffer_rsrc_t rs, u32 s0, u32 s1, u32 s2, u32 s3) {
  asm volatile(
      "s_nop 4\n\t"
      "buffer_load_dwordx4 %0, %8, %9, %10 offen sc1\n\t"
      "buffer_load_dwordx4 %1, %8, %9, %10 offen offset:1024 sc1\n\t"
      "buffer_load_dwordx4 %2, %8, %9, %11 offen sc1\n\t"
      "buffer_load_dwordx4 %3, %8, %9, %11 offen offset:1024 sc1\n\t"
      "buffer_load_dwordx4 %4, %8, %9, %12 offen sc1\n\t"
      "buffer_load_dwordx4 %5, %8, %9, %12 offen offset:1024 sc1\n\t"
      "buffer_load_dwordx4 %6, %8, %9, %13 offen sc1\n\t"
      "buffer_load_dwordx4 %7, %8, %9, %13 offen offset:1024 sc1\n\t"
      "s_waitcnt vmcnt(0)"
      : "=&v"(v[0]), "=&v"(v[1]), "=&v"(v[2]), "=&v"(v[3]), "=&v"(v[4]), "=&v"(v[5]), "=&v"(v[6]), "=&v"(v[7])
      : "v"(vo), "s"(rs), "s"(s0), "s"(s1), "s"(s2), "s"(s3)
      : "memory");
}
DI void poll_eq(GAS u32* p, u32 want) {
  for (u32 spins = 0; spins < SPIN_CAP; ++spins) {
    const u32 v = __hip_atomic_load(p, RLX_AGENT);
    if (__all(v == want)) break;
    __builtin_amdgcn_s_sleep(1);
  }
}
DI bool all_written(const v4u (&v)[8]) {
  u32 mx = 0;
#pragma unroll
  for (int i = 0; i < 8; ++i) {
    const u32 a = v[i][0] > v[i][1] ? v[i][0] : v[i][1], c = v[i][2] > v[i][3] ? v[i][2] : v[i][3];
    const u32 d = a > c ? a : c;
    mx = mx > d ? mx : d;
  }
  return __all(mx != 0xffffffffu);
}
}

__global__ __launch_bounds__(512, 2) void gqa_block_kernel(const char* __restrict__ x, const char* __restrict__ kc, const char* __restrict__ vc,
                                                           const char* __restrict__ wq, const char* __restrict__ wo, char* __restrict__ out,
                                                           char* wsp) {
  extern __shared__ __attribute__((aligned(16))) char smem_[];
  const int w = blockIdx.x;
  const int wave = rfl(threadIdx.x >> 6);
  const int lane = threadIdx.x & 63;
  const int j = w & 7, g = w >> 5, hh = (w >> 3) & 3;
  const int h = 4 * j + hh;
  const int hrot = hh + g;
  GAS char* ws = (GAS char*)wsp;
  const u32 lbase = (u32)(size_t)(LAS char*)smem_;

  auto loader = [&]() __attribute__((always_inline)) {
    const int lw = wave - 4;
    __builtin_amdgcn_s_setprio(1);
    const __amdgpu_buffer_rsrc_t rX = __builtin_amdgcn_make_buffer_rsrc((void*)x, 0, 512 * 1280, 0x00020000);
    const __amdgpu_buffer_rsrc_t rWq = __builtin_amdgcn_make_buffer_rsrc((void*)wq, 0, 640 * 12288, 0x00020000);
    u32 ra[2], ca[2], vA[2], vKV[2];
#pragma unroll
    for (int i = 0; i < 2; ++i) {
      ra[i] = 8 * i + (lane >> 3);
      ca[i] = 16 * ((lane & 7) ^ ((ra[i] >> 1) & 7));
      vA[i] = ra[i] * 1280 + ca[i];
    }
    u32 kk[6], cc[6], vW[6];
#pragma unroll
    for (int i = 0; i < 6; ++i) {
      const u32 L = 8 * i + (lane >> 3), k = (L * 43) >> 7;
      kk[i] = k;
      cc[i] = 128 * (L - 3 * k) + 16 * ((lane & 7) ^ ((k & 2) << 1) ^ ((k & 8) >> 2));
      vW[i] = k * 12288 + cc[i];
    }
#pragma unroll
    for (int i = 0; i < 2; ++i) vKV[i] = ra[i] * 384 + ca[i];
    const u32 ldsW = lbase + RING + lw * 6144, ldsA = lbase + RING + ACT + lw * 2048;
    const u32 soW0 = h * 384 + 16 * lw * 12288, soA0 = (64 * g + 16 * lw) * 1280;
    const int bl = 2 * g + (lw >> 1);
    const u32 soKV0 = (u32)(bl * 32 * 32 + 16 * (lw & 1)) * 384;
    const u32 L0 = (5 * hh) >> 1;
    int kcur = (g + 3 * hh) % 10;
    const int crot = g >> 2;
    const int kpos = ((w >> 3) + j) & 7;
    const __amdgpu_buffer_rsrc_t rWs = __builtin_amdgcn_make_buffer_rsrc((void*)wsp, 0, WS_USED, 0x00020000);
    u32 Ev = 0;
    auto after_barrier = [&](int s) {
      if (s == 0) {
        if (lw == 0) dma_word(0, rWs, WS_E + g * 128, lbase + SCR);
        asm volatile("buffer_load_dword %0, %1, %2, %3 offen sc1" : "+v"(Ev) : "v"(0u), "s"(rWs), "s"(WS_E + g * 128) : "memory");
        const v4u ones = {~0u, ~0u, ~0u, ~0u};
        asm volatile("buffer_store_dwordx4 %0, %1, %2, %3 offen sc1\n\ts_nop 1" ::"v"(ones), "v"(lane * 16 + 1024 * (lw & 1)), "s"(rWs),
                     "s"(WS_P + (bl * 32 + h) * 2048)
                     : "memory");
      }
      if (s == 1) {
        asm volatile("" : "+v"(Ev));
        const u32 ev = Ev + 1;
        if (lane == 0)
          asm volatile("buffer_store_dword %0, %1, %2, %3 offen sc1\n\ts_nop 1" ::"v"(ev), "v"(0u), "s"(rWs),
                       "s"(WS_FLAGP + ((bl * 32 + h) * 2 + (lw & 1)) * 128)
                       : "memory");
      }
      if (s == FDMA_S && lw == 0)
        dma_word((((lane >> 3) & 1) * 64 + (lane & 7)) * 128, rWs, WS_FLAGP + ((2 * g * 32 + 4 * j) * 2) * 128, lbase + KREG + 16384);
      if (s == PDMA_S) {
#pragma unroll
        for (int i = 0; i < 2; ++i)
#pragma unroll
          for (int qb = 0; qb < 2; ++qb) {
            const int hx = 2 * (lw & 1) + i;
            dma<true>(lane * 16, rWs, WS_P + (bl * 32 + 4 * j + ((hrot + hx) & 3)) * 2048 + 1024 * qb,
                      lbase + KREG + (((lw >> 1) * 4 + hx) * 2 + qb) * 1024);
          }
      }
      if (s < 8 && s == kpos) {
        const __amdgpu_buffer_rsrc_t rK = __builtin_amdgcn_make_buffer_rsrc((void*)kc, 0, 512 * 12288, 0x00020000);
#pragma unroll
        for (int c = 0; c < 3; ++c)
#pragma unroll
          for (int i = 0; i < 2; ++i) dma<false>(vKV[i], rK, soKV0 + h * 12288 + 128 * c, lbase + KREG + 8192 * c + lw * 2048 + 1024 * i);
      }
    };
    static_for<0, NST>([&](auto tc_) {
      constexpr int t = decltype(tc_)::value;
      constexpr u32 slot = (t % NSLOT) * SLOT;
      u32 soW, soA;
      if constexpr (t < 10) {
        soW = soW0 + kcur * (64 * 12288);
        soA = soA0 + kcur * 128;
        kcur = kcur == 9 ? 0 : kcur + 1;
      } else {
        if constexpr (t == 10) {
#pragma unroll
          for (int i = 0; i < 2; ++i) vA[i] = vKV[i];
#pragma unroll
          for (int i = 0; i < 6; ++i) vW[i] = kk[i] * 1280 + cc[i];
        }
        constexpr int hi = (t - 10) / 3, c0 = (t - 10) % 3;
        const int c = c0 + crot >= 3 ? c0 + crot - 3 : c0 + crot;
        const u32 hp = 4 * j + ((hrot + hi) & 3);
        soW = (hp * 192 + 64 * c + 16 * lw) * 1280 + 128 * L0;
        soA = soKV0 + hp * 12288 + 128 * c;
      }
      static_for<0, 8>([&](auto ic_) {
        constexpr int i = decltype(ic_)::value, p = 8 * t + i;
        if constexpr (t < 10) {
          if constexpr (i < 2) dma<false>(vA[i], rX, soA, slot + ldsA + 1024 * i);
          else dma<false>(vW[i - 2], rWq, soW, slot + ldsW + 1024 * (i - 2));
        } else {
          const __amdgpu_buffer_rsrc_t rV = __builtin_amdgcn_make_buffer_rsrc((void*)vc, 0, 512 * 12288, 0x00020000);
          const __amdgpu_buffer_rsrc_t rWo = __builtin_amdgcn_make_buffer_rsrc((void*)wo, 0, 6144 * 1280, 0x00020000);
          if constexpr (i < 2) dma<false>(vA[i], rV, soA, slot + ldsA + 1024 * i);
          else dma<false>(vW[i - 2], rWo, soW, slot + ldsW + 1024 * (i - 2));
        }
        if constexpr (p + 1 - LA >= 8 && (p + 1 - LA) % 8 == 0) {
          constexpr int s = (p + 1 - LA) / 8 - 1;
          wait_vm<LA>();
          bar();
          if constexpr (s < 8 || s == FDMA_S || s == PDMA_S) after_barrier(s);
        }
      });
    });
    static_for<(8 * NST - LA) / 8, NST>([&](auto sc_) {
      constexpr int s = decltype(sc_)::value;
      wait_vm<8 * (NST - 1 - s)>();
      bar();
    });
  };

  auto compute = [&]() __attribute__((always_inline)) {
    const int cw = wave, b = cw >> 1, half = cw & 1;
    const __amdgpu_buffer_rsrc_t rWs = __builtin_amdgcn_make_buffer_rsrc((void*)wsp, 0, WS_USED, 0x00020000);
    const int r16 = lane & 15, g4 = lane >> 4, q4 = (lane & 15) >> 2, p4 = lane & 3;
    const int x2 = (g4 & 1) | ((q4 >> 1) << 1);
    const u32 fsw = (r16 >> 1) & 7;
    u32 e = 0, Ev = 0;
    f32x4 accS[2][2];
    {
      f32x4 accQ[6][2];
      s16x4 kl[3][2], kh[3][2];
      u32 aW[6], aX[2], aY[2], aK[3][2];
#pragma unroll
      for (int tb = 0; tb < 6; ++tb) aW[tb] = lbase + RING + 384 * (8 * g4 + q4) + 32 * ((6 * half + tb) ^ x2) + 16 * (p4 >> 1) + 8 * (p4 & 1);
#pragma unroll
      for (int ks = 0; ks < 2; ++ks) {
        aX[ks] = lbase + RING + ACT + 128 * (32 * b + 16 * half + r16) + 16 * ((4 * ks + g4) ^ fsw);
        aY[ks] = lbase + RING + ACT + 128 * (32 * b + 16 * (half ^ 1) + r16) + 16 * ((4 * ks + g4) ^ fsw);
      }
#pragma unroll
      for (int u = 0; u < 3; ++u)
#pragma unroll
        for (int a = 0; a < 2; ++a) {
          const int ch = 12 * half + 4 * u + 2 * a + (g4 >> 1);
          aK[u][a] = lbase + KREG + 8192 * (ch >> 3) + 128 * (32 * b + r16) + 16 * ((ch & 7) ^ fsw) + 8 * (g4 & 1);
        }
#pragma unroll
      for (int i = 0; i < 6; ++i) asm volatile("" : "+v"(aW[i]));
#pragma unroll
      for (int i = 0; i < 2; ++i) asm volatile("" : "+v"(aX[i]), "+v"(aY[i]));
#pragma unroll
      for (int i = 0; i < 6; ++i) asm volatile("" : "+v"(aK[i >> 1][i & 1]));

      s16x4 wl[2][6], wh[2][6];
      bf16x8 xf[2][2];
      auto rd1 = [&](int n, int part) {
        const int set = n & 1, ks = n & 1;
        if (part == 0) xf[set][0] = lds128(aX[ks], 0);
        else if (part == 3) xf[set][1] = lds128(aY[ks], 0);
        else {
          const int q = part < 3 ? part - 1 : part - 2, tb = q >> 1;
          if (q & 1) wh[set][tb] = ldstr4(aW[tb], 384 * (32 * ks + 4));
          else wl[set][tb] = ldstr4(aW[tb], 384 * 32 * ks);
        }
      };
      auto rdk = [&](int m) {
        const int u = m >> 2, kb = (m >> 1) & 1, a = m & 1;
        const s16x4 v = *(LAS s16x4*)(lp(aK[u][a]) + 2048 * kb);
        if (a) kh[u][kb] = v; else kl[u][kb] = v;
      };
      auto adv1 = [&](int s) {
        const u32 d = (s % NSLOT == 0) ? (u32)(-(NSLOT - 1) * SLOT) : (u32)SLOT;
#pragma unroll
        for (int i = 0; i < 6; ++i) { aW[i] += d; asm volatile("" : "+v"(aW[i])); }
#pragma unroll
        for (int i = 0; i < 2; ++i) { aX[i] += d; aY[i] += d; asm volatile("" : "+v"(aX[i]), "+v"(aY[i])); }
      };
      auto mm1 = [&](int n, int tb, int qb) {
        const int set = n & 1;
        const f32x4 c0 = (n == 0) ? (f32x4){0.f, 0.f, 0.f, 0.f} : accQ[tb][qb];
        accQ[tb][qb] = mfma16(cat8(wl[set][tb], wh[set][tb]), xf[set][qb], c0);
      };
      bar();
#pragma unroll
      for (int q = 0; q < 14; ++q) rd1(0, q);
      SB();
#pragma unroll
      for (int n = 0; n < 19; ++n) {
        const int s = n >> 1, ks = n & 1;
        if (ks == 1) {
          wait_lgkm0();
          bar();
        }
#pragma unroll
        for (int m = 0; m < 12; ++m) {
          mm1(n, m >> 1, m & 1);
          SB();
#pragma unroll
          for (int q = 0; q < 14; ++q)
            if (q >= rsched1(ks, m) && q < rsched1(ks, m + 1)) rd1(n + 1, q);
          if (ks == 0 && s < 9 && m == 9) adv1(s + 1);
          SB();
        }
        if (n == EPO_N - 2) Ev = *(LAS u32*)(lp(lbase + SCR));
        if (n == EPO_N) {
          e = rfl(Ev) + 1;
          asm volatile("" : "+s"(e));
        }
        SB();
      }
      v4u qp[2];
      auto qpack = [&](int u, int qb) {
        qp[qb][0] = pk2(accQ[2 * u][qb][0], accQ[2 * u][qb][1]);
        qp[qb][1] = pk2(accQ[2 * u][qb][2], accQ[2 * u][qb][3]);
        qp[qb][2] = pk2(accQ[2 * u + 1][qb][0], accQ[2 * u + 1][qb][1]);
        qp[qb][3] = pk2(accQ[2 * u + 1][qb][2], accQ[2 * u + 1][qb][3]);
      };
      auto smm = [&](int u) {
#pragma unroll
        for (int qb = 1; qb >= 0; --qb)
#pragma unroll
          for (int kb = 0; kb < 2; ++kb) {
            const f32x4 c0 = u == 0 ? (f32x4){0.f, 0.f, 0.f, 0.f} : accS[kb][qb];
            accS[kb][qb] = mfma16(cat8(kl[u][kb], kh[u][kb]), __builtin_bit_cast(bf16x8, qp[qb]), c0);
          }
      };
#pragma unroll
      for (int m = 0; m < 12; ++m) {
        mm1(19, m >> 1, m & 1);
        SB();
        if (m < 6) { rdk(2 * m); rdk(2 * m + 1); }
        if (m == 5) qpack(0, 0);
        if (m == 6) qpack(0, 1);
        if (m == 7) smm(0);
        if (m == 9) qpack(1, 0);
        if (m == 10) qpack(1, 1);
        if (m == 11) smm(1);
        SB();
      }
      qpack(2, 0);
      qpack(2, 1);
      smm(2);
      const u32 scw = lbase + SCR + 4096 * b + 2048 * half + 16 * lane;
#pragma unroll
      for (int kb = 0; kb < 2; ++kb) *(LAS f32x4*)(lp(scw) + 1024 * kb) = accS[kb][1];
      wait_lgkm0();
    }
    const int c016 = (hh & 1) * 2;
    u32 aW[5], aV[2];
#pragma unroll
    for (int cb = 0; cb < 5; ++cb)
      aW[cb] = lbase + RING + SLOT * (10 % NSLOT) + 384 * (8 * g4 + q4) + 32 * ((c016 + 5 * half + cb) ^ x2) + 16 * (p4 >> 1) + 8 * (p4 & 1);
#pragma unroll
    for (int ks = 0; ks < 2; ++ks) aV[ks] = lbase + RING + SLOT * (10 % NSLOT) + ACT + 128 * (32 * b + r16) + 16 * ((4 * ks + g4) ^ fsw);
#pragma unroll
    for (int i = 0; i < 5; ++i) asm volatile("" : "+v"(aW[i]));
#pragma unroll
    for (int i = 0; i < 2; ++i) asm volatile("" : "+v"(aV[i]));
    bar();
    s16x4 wl[2][5], wh[2][5];
    bf16x8 vf[2][2];
    auto rd2 = [&](int n, int part) {
      const int set = n & 1, ks = n & 1;
      if (part == 0) vf[set][0] = lds128(aV[ks], 0);
      else if (part == 3) vf[set][1] = lds128(aV[ks], 2048);
      else {
        const int q = part < 3 ? part - 1 : part - 2, cb = q >> 1;
        if (q & 1) wh[set][cb] = ldstr4(aW[cb], 384 * (32 * ks + 4));
        else wl[set][cb] = ldstr4(aW[cb], 384 * 32 * ks);
      }
    };
    auto adv2 = [&](int s) {
      const u32 d = (s % NSLOT == 0) ? (u32)(-(NSLOT - 1) * SLOT) : (u32)SLOT;
#pragma unroll
      for (int i = 0; i < 5; ++i) { aW[i] += d; asm volatile("" : "+v"(aW[i])); }
#pragma unroll
      for (int i = 0; i < 2; ++i) { aV[i] += d; asm volatile("" : "+v"(aV[i])); }
    };
    f32x4 X[2][5];
    v4u Xp[4][5];
    v4u Pf[8];
    f32x4 O[5][2];
#pragma unroll
    for (int cb = 0; cb < 5; ++cb)
#pragma unroll
      for (int qb = 0; qb < 2; ++qb) O[cb][qb] = (f32x4){0.f, 0.f, 0.f, 0.f};
    auto mm2 = [&](int n, int cb, int rb) {
      const int set = n & 1;
      const f32x4 c0 = (n % 6 == 0) ? (f32x4){0.f, 0.f, 0.f, 0.f} : X[rb][cb];
      X[rb][cb] = mfma16(vf[set][rb], cat8(wl[set][cb], wh[set][cb]), c0);
    };
    auto pack = [&](int hi, int cb) {
      Xp[hi][cb][0] = pk2(X[0][cb][0], X[0][cb][1]);
      Xp[hi][cb][1] = pk2(X[0][cb][2], X[0][cb][3]);
      Xp[hi][cb][2] = pk2(X[1][cb][0], X[1][cb][1]);
      Xp[hi][cb][3] = pk2(X[1][cb][2], X[1][cb][3]);
      asm volatile("" : "+v"(Xp[hi][cb]));
    };
    auto pvi = [&](int k) {
      const int hi = k / 10, cb = (k % 10) >> 1, qb = k & 1;
      O[cb][qb] = mfma16(__builtin_bit_cast(bf16x8, Xp[hi][cb]), __builtin_bit_cast(bf16x8, Pf[2 * hi + qb]), O[cb][qb]);
    };
    float sv[8], mc = 0.f, sum = 0.f, t0 = 0.f, t1 = 0.f;
    v4u pfo;
    constexpr float scale = 0.07216878364870322f, cs = 1.4426950408889634f;
    {
      const u32 scr = lbase + SCR + 4096 * b + 2048 * (half ^ 1) + 16 * lane;
      const f32x4 o0 = *(LAS f32x4*)(lp(scr)), o1 = *(LAS f32x4*)(lp(scr) + 1024);
      SB();
#pragma unroll
      for (int q = 0; q < 12; ++q) rd2(0, q);
      SB();
#pragma unroll
      for (int r = 0; r < 4; ++r) {
        sv[r] = accS[0][0][r] + o0[r];
        sv[4 + r] = accS[1][0][r] + o1[r];
      }
#pragma unroll
      for (int d = 0; d < 4; ++d) {
        const u32 a = pk2(sv[2 * d], sv[2 * d + 1]);
        const u32 c = pk2(__uint_as_float(a << 16) * scale, __uint_as_float(a & 0xffff0000u) * scale);
        sv[2 * d] = __uint_as_float(c << 16);
        sv[2 * d + 1] = __uint_as_float(c & 0xffff0000u);
      }
      float m = fmaxf(fmaxf(fmaxf(sv[0], sv[1]), fmaxf(sv[2], sv[3])), fmaxf(fmaxf(sv[4], sv[5]), fmaxf(sv[6], sv[7])));
      const auto s1 = __builtin_amdgcn_permlane16_swap(__float_as_uint(m), __float_as_uint(m), false, false);
      m = fmaxf(__uint_as_float(s1[0]), __uint_as_float(s1[1]));
      const auto s2 = __builtin_amdgcn_permlane32_swap(__float_as_uint(m), __float_as_uint(m), false, false);
      m = fmaxf(__uint_as_float(s2[0]), __uint_as_float(s2[1]));
      mc = -m * cs;
    }
    SB();
    auto smx = [&](int n, int m) {
      if (n == 0) {
        if (m == 0 || m == 1) {
#pragma unroll
          for (int r = 0; r < 4; ++r) sv[4 * m + r] = __builtin_fmaf(sv[4 * m + r], cs, mc);
        } else if (m <= 5) {
          sv[2 * (m - 2)] = __builtin_amdgcn_exp2f(sv[2 * (m - 2)]);
          sv[2 * (m - 2) + 1] = __builtin_amdgcn_exp2f(sv[2 * (m - 2) + 1]);
        } else if (m == 6) {
          t0 = (sv[0] + sv[1]) + (sv[2] + sv[3]);
        } else if (m == 7) {
          t1 = (sv[4] + sv[5]) + (sv[6] + sv[7]);
          sum = t0 + t1;
        } else if (m == 8) {
          const auto s1 = __builtin_amdgcn_permlane16_swap(__float_as_uint(sum), __float_as_uint(sum), false, false);
          sum = __uint_as_float(s1[0]) + __uint_as_float(s1[1]);
        } else {
          const auto s2 = __builtin_amdgcn_permlane32_swap(__float_as_uint(sum), __float_as_uint(sum), false, false);
          sum = __builtin_amdgcn_rcpf(__uint_as_float(s2[0]) + __uint_as_float(s2[1]));
        }
      } else if (n == 1) {
        if (m == 1 || m == 2) {
#pragma unroll
          for (int r = 0; r < 4; ++r) sv[4 * (m - 1) + r] *= sum;
        } else if (m == 3) {
#pragma unroll
          for (int d = 0; d < 4; ++d) pfo[d] = pk2(sv[2 * d], sv[2 * d + 1]);
        } else if (m == 4) {
#pragma unroll
          for (int d = 0; d < 4; ++d) pfo[d] = pfo[d] < 0xfffffffeu ? pfo[d] : 0xfffffffeu;
        } else if (m == 5) {
          store16_sc1(pfo, rWs, lane * 16 + 1024 * half, WS_P + ((2 * g + b) * 32 + h) * 2048);
        }
      }
    };
    u32 fval = 0;
    static_for<0, 24>([&](auto nc_) {
      constexpr int n = decltype(nc_)::value;
      constexpr int s = 10 + (n >> 1), ks = n & 1, hi = n / 6;
      constexpr bool last = (n % 6 == 5);
      if (ks == 1 && s < NST - 1) {
        wait_lgkm0();
        bar();
      }
#pragma unroll
      for (int m = 0; m < 10; ++m) {
        mm2(n, m >> 1, m & 1);
        SB();
        if (n + 1 < 24) {
#pragma unroll
          for (int q = 0; q < 12; ++q)
            if (q >= rsched2(ks, m) && q < rsched2(ks, m + 1)) rd2(n + 1, q);
        }
        if (n < 2) smx(n, m);
        if (last) {
          if (m == 4) pack(hi, 0);
          if (m == 6) pack(hi, 1);
          if (m == 8) pack(hi, 2);
          if (m == 9) { pack(hi, 3); pack(hi, 4); }
        } else if (ks == 0 && s < NST - 1 && m == 8) adv2(s + 1);
        SB();
      }
      if (n == PCHK_N - 1) {
        fval = *(LAS u32*)(lp(lbase + KREG + 16384 + 4 * (8 * b + (lane & 7))));
#pragma unroll
        for (int i = 0; i < 8; ++i) Pf[i] = *(LAS v4u*)(lp(lbase + KREG + 16 * lane) + (b * 8 + i) * 1024);
      }
      if (n == PCHK_N) {
        asm volatile("" : "+v"(X[0][0]), "+v"(X[0][1]), "+v"(X[0][2]), "+v"(X[0][3]), "+v"(X[0][4]), "+v"(X[1][0]), "+v"(X[1][1]), "+v"(X[1][2]),
                     "+v"(X[1][3]), "+v"(X[1][4]));
        if (!(__all(fval == e) && all_written(Pf))) {
          poll_eq((GAS u32*)(ws + WS_FLAGP + (((2 * g + b) * 32 + 4 * j) * 2 + (lane & 7)) * 128), e);
          asm volatile("" ::: "memory");
          const u32 pb0 = WS_P + ((2 * g + b) * 32 + 4 * j) * 2048;
          u32 spins = 0;
          do {
            load4x2_sc1(Pf, lane * 16, rWs, pb0 + (hrot & 3) * 2048, pb0 + ((hrot + 1) & 3) * 2048, pb0 + ((hrot + 2) & 3) * 2048,
                        pb0 + ((hrot + 3) & 3) * 2048);
          } while (!all_written(Pf) && ++spins < SPIN_CAP);
        }
      }
#pragma unroll
      for (int k = 0; k < 40; ++k)
        if (k >= pvsched(n) && k < pvsched(n + 1)) pvi(k);
      SB();
    });
    {
      const u32 pos = (r16 >> 1) * 8 + (r16 & 1) * 4 + g4;
      const u32 sb = WS_SLAB + (w * 4 + cw) * 5120;
#pragma unroll
      for (int cb = 0; cb < 5; ++cb) {
        v4u v;
        v[0] = pk2(O[cb][0][0], O[cb][0][1]); v[1] = pk2(O[cb][0][2], O[cb][0][3]);
        v[2] = pk2(O[cb][1][0], O[cb][1][1]); v[3] = pk2(O[cb][1][2], O[cb][1][3]);
        store16_sc1(v, rWs, pos * 16 + 1024 * cb, sb);
      }
      wait_vm<0>();
      if (lane == 0) __hip_atomic_store((GAS u32*)(ws + WS_FLAGO + (w * 4 + cw) * 128), e, RLX_AGENT);
    }
    {
      const int w0 = w & ~7;
      const int cb = lane < 40 ? lane >> 3 : 4, rr = lane & 7;
      poll_eq((GAS u32*)(ws + WS_FLAGO + ((w0 + (lane & 7)) * 4 + cw) * 128), e);
      asm volatile("" ::: "memory");
      v4u v[8];
      load8_sc1(v, (8 * j + rr) * 16 + 1024 * cb, rWs, WS_SLAB + (w0 * 4 + cw) * 5120, 4 * 5120);
      if (lane < 40) {
        float a[8];
#pragma unroll
        for (int d = 0; d < 4; ++d) {
          float lo[8], hi[8];
#pragma unroll
          for (int i = 0; i < 8; ++i) {
            lo[i] = __uint_as_float(v[i][d] << 16);
            hi[i] = __uint_as_float(v[i][d] & 0xffff0000u);
          }
          a[2 * d] = ((lo[0] + lo[1]) + (lo[2] + lo[3])) + ((lo[4] + lo[5]) + (lo[6] + lo[7]));
          a[2 * d + 1] = ((hi[0] + hi[1]) + (hi[2] + hi[3])) + ((hi[4] + hi[5]) + (hi[6] + hi[7]));
        }
        const int qp = 2 * j + (rr >> 2), gg = rr & 3;
        const u32 col = 160 * hh + 80 * half + 16 * cb + 4 * gg;
        GAS char* o0 = (GAS char*)out + (size_t)((2 * g + b) * 32 + qp) * 1280 + col * 2;
        *(GAS v2u*)o0 = (v2u){pk2(a[0], a[1]), pk2(a[2], a[3])};
        *(GAS v2u*)(o0 + 16 * 1280) = (v2u){pk2(a[4], a[5]), pk2(a[6], a[7])};
      }
      if (j == 0 && hh == 0 && cw == 0 && lane == 0) __hip_atomic_store((GAS u32*)(ws + WS_E + g * 128), e, RLX_AGENT);
    }
  };

  if (wave < 4) compute(); else loader();
}

extern "C" void kernel_launch(void* const* d_in, const int* in_sizes, int n_in, void* d_out, int out_size, void* d_ws, size_t ws_size,
                              hipStream_t stream) {
  (void)hipFuncSetAttribute((const void*)gqa_block_kernel, hipFuncAttributeMaxDynamicSharedMemorySize, LDS_BYTES);
  gqa_block_kernel<<<256, 512, LDS_BYTES, stream>>>((const char*)d_in[0], (const char*)d_in[1], (const char*)d_in[2], (const char*)d_in[3],
                                                     (const char*)d_in[4], (char*)d_out, (char*)d_ws);
}
```
